```python
import torch

def _softmax(x, axis=-1):
    return torch.softmax(x, dim=axis)
def _gelu(x):
    return torch.nn.functional.gelu(x, approximate="tanh")
def _silu(x):
    return torch.nn.functional.silu(x)
def _sigmoid(x):
    return torch.sigmoid(x)
def _layernorm(x, axis=-1, eps=1e-5):
    m = x.mean(dim=axis, keepdim=True)
    v = x.var(dim=axis, keepdim=True, unbiased=False)
    return (x - m) / torch.sqrt(v + eps)
def _scatter_add(tgt, idx, src, axis=0):
    return tgt.clone().index_add_(axis, idx, src)
def _segment_sum(src, seg, n):
    out = torch.zeros((n,) + src.shape[1:], dtype=src.dtype)
    return out.index_add_(0, seg, src)
def _pad(x, pw):
    flat = tuple(v for pair in reversed(pw) for v in pair)
    return torch.nn.functional.pad(x, flat)

kernel_name = 'gated_mix__mha_block_cached__glu_gated_bilinear__decode'

def setup_inputs():

    _w = (lambda a: (lambda t: t.to(torch.bfloat16) if t.is_floating_point() else t)(torch.as_tensor(a)))
    def _A_setup():
        B, Sq, D = 1, 1, 512
        H, Sk, Dh = 32, 8192, 128
        return {
            "x": _w((lambda *s: torch.randn(*s, dtype=torch.bfloat16) * 0.05)(B, Sq, D)),
            "k_cache": _w((lambda *s: torch.randn(*s, dtype=torch.bfloat16) * 3.2)(B, H, Sk, Dh)),
            "v_cache": _w((lambda *s: torch.randn(*s, dtype=torch.bfloat16) * 0.2)(B, H, Sk, Dh)),
            "wq": _w((lambda *s: torch.randn(*s, dtype=torch.bfloat16) * 3.2)(D, H * Dh) * (D ** -0.5)),
            "wo": _w((lambda *s: torch.randn(*s, dtype=torch.bfloat16) * 0.2)(H * Dh, D) * ((H * Dh) ** -0.5)),
        }
    _A_in = _A_setup()

    def _B_setup():
        B, S, D, F = 1, 1, 512, 1376
        return {
            "x": _w((lambda *s: torch.randn(*s, dtype=torch.bfloat16) * 0.05)(B, S, D)),
            "w_gate": _w((lambda *s: torch.randn(*s, dtype=torch.bfloat16) * 0.8)(D, F) * (D ** -0.5)),
            "w_up": _w((lambda *s: torch.randn(*s, dtype=torch.bfloat16) * 0.8)(D, F) * (D ** -0.5)),
            "w_down": _w((lambda *s: torch.randn(*s, dtype=torch.bfloat16) * 0.8)(F, D) * (F ** -0.5)),
        }
    _B_in = _B_setup()

    return {
        "A_x": _A_in["x"],
        "A_k_cache": _A_in["k_cache"],
        "A_v_cache": _A_in["v_cache"],
        "A_wq": _A_in["wq"],
        "A_wo": _A_in["wo"],
        "B_x": _B_in["x"],
        "B_w_gate": _B_in["w_gate"],
        "B_w_up": _B_in["w_up"],
        "B_w_down": _B_in["w_down"],
        "gate_w": _w((lambda *s: torch.randn(*s, dtype=torch.bfloat16) * 0.1)(512) * 16.0),
    }

def reference(A_x, A_k_cache, A_v_cache, A_wq, A_wo, B_x, B_w_gate, B_w_up, B_w_down, gate_w):
    def _A_ref(x, k_cache, v_cache, wq, wo):
        B, Sq, D = x.shape
        H, Dh = 32, 128
        q = (lambda x, *d: x.permute(*d))((x @ wq).reshape(B, Sq, H, Dh), 0, 2, 1, 3)
        scores = torch.einsum("bhqd,bhkd->bhqk", q, k_cache) * (Dh ** -0.5)
        p = _softmax(scores, axis=-1)
        o = torch.einsum("bhqk,bhkd->bhqd", p, v_cache)
        return (lambda x, *d: x.permute(*d))(o, 0, 2, 1, 3).reshape(B, Sq, H * Dh) @ wo

    def _B_ref(x, w_gate, w_up, w_down):
        g = torch.einsum("bsd,df->bsf", x, w_gate)
        u = torch.einsum("bsd,df->bsf", x, w_up)
        h = g * u
        return torch.einsum("bsf,fd->bsd", h, w_down)

    x = A_x
    g = _sigmoid((x * gate_w).sum(-1, keepdims=True))
    a = _A_ref(x, A_k_cache, A_v_cache, A_wq, A_wo)
    b = _B_ref(x, B_w_gate, B_w_up, B_w_down)
    return g * a + (1 - g) * b
```

```cpp
#include <hip/hip_runtime.h>
#include <hip/hip_bf16.h>
#include <stdint.h>

typedef unsigned u32x4 __attribute__((ext_vector_type(4)));
typedef unsigned u32x2 __attribute__((ext_vector_type(2)));
typedef float f32x2 __attribute__((ext_vector_type(2)));
typedef __bf16 bf16x2 __attribute__((ext_vector_type(2)));
#define GAS __attribute__((address_space(1)))
typedef const GAS u32x4* gp4;

constexpr int D = 512, NH = 32, SK = 8192, DH = 128, FF = 1376;
constexpr int NWAVE = 8, NTHR = 512, NWG = 256;
constexpr int KEYS_WG = 1024, KEYS_WAVE = 128;
constexpr int WS_EPOCH = 0;
constexpr int WS_H0 = 4096;
constexpr int WS_H1 = WS_H0 + NWG * 1024;
constexpr int WS_H2 = WS_H1 + NWG * 2048;
constexpr int WS_BYTES = WS_H2 + NWG * 1024;
constexpr int AUX_SC1 = 16;
constexpr unsigned SPIN_CAP = 1u << 18;

__device__ __forceinline__ float dot2(unsigned a, unsigned b, float c) {
  return __builtin_amdgcn_fdot2_f32_bf16(__builtin_bit_cast(bf16x2, a), __builtin_bit_cast(bf16x2, b), c, false);
}
__device__ __forceinline__ float bflo(unsigned u) { return __builtin_bit_cast(float, u << 16); }
__device__ __forceinline__ float bfhi(unsigned u) { return __builtin_bit_cast(float, u & 0xffff0000u); }
__device__ __forceinline__ unsigned pack_bf16(float lo, float hi) {
  bf16x2 r; r.x = (__bf16)lo; r.y = (__bf16)hi; return __builtin_bit_cast(unsigned, r);
}
__device__ __forceinline__ float rbf(float x) { return (float)(__bf16)x; }
template <int CTRL> __device__ __forceinline__ float dpp(float x) {
  return __builtin_bit_cast(float, __builtin_amdgcn_mov_dpp(__builtin_bit_cast(int, x), CTRL, 0xf, 0xf, true));
}
constexpr int XOR1 = 0xB1, XOR2 = 0x4E, XOR7 = 0x141, XOR8 = 0x128, ROR4 = 0x124, ROR2 = 0x122, ROR1 = 0x121;
__device__ __forceinline__ unsigned f2u(float x) { return __builtin_bit_cast(unsigned, x); }
__device__ __forceinline__ float u2f(unsigned x) { return __builtin_bit_cast(float, x); }
__device__ __forceinline__ float tr32(float a, float b) {
  auto r = __builtin_amdgcn_permlane32_swap(f2u(a), f2u(b), false, false);
  return u2f(r[0]) + u2f(r[1]);
}
__device__ __forceinline__ float tr16(float a, float b) {
  auto r = __builtin_amdgcn_permlane16_swap(f2u(a), f2u(b), false, false);
  return u2f(r[0]) + u2f(r[1]);
}
__device__ __forceinline__ float xrow_sum(float x) { return tr32(tr16(x, x), tr16(x, x)); }
__device__ __forceinline__ float xrow_max(float x) {
  auto s = __builtin_amdgcn_permlane16_swap(f2u(x), f2u(x), false, false);
  x = fmaxf(u2f(s[0]), u2f(s[1]));
  auto t = __builtin_amdgcn_permlane32_swap(f2u(x), f2u(x), false, false);
  return fmaxf(u2f(t[0]), u2f(t[1]));
}
__device__ __forceinline__ float row_sum(float x) {
  x += dpp<XOR1>(x); x += dpp<XOR2>(x); x += dpp<XOR7>(x); x += dpp<XOR8>(x); return x;
}
__device__ __forceinline__ float row_max(float x) {
  x = fmaxf(x, dpp<XOR1>(x)); x = fmaxf(x, dpp<XOR2>(x)); x = fmaxf(x, dpp<XOR7>(x)); x = fmaxf(x, dpp<XOR8>(x)); return x;
}
constexpr int AUX_NT = 2;
__device__ __forceinline__ u32x4 ld_kv(__amdgpu_buffer_rsrc_t r, int voff, int i) {
  return __builtin_amdgcn_raw_buffer_load_b128(r, voff + (i & 3) * 1024, (i >> 2) * 4096, AUX_NT);
}
__device__ __forceinline__ u32x4 ld_gran(__amdgpu_buffer_rsrc_t r, int voff, int soff) {
  asm volatile("" : "+v"(voff)::"memory");
  return __builtin_amdgcn_raw_buffer_load_b128(r, voff, soff, AUX_SC1);
}
#define SCHED_FENCE() __builtin_amdgcn_sched_barrier(0)

template <int I> struct PB {
  __device__ static __forceinline__ float get(float x) { return dpp<0x150 + I>(x); }
};

template <int I> __device__ __forceinline__ void pv_step(const u32x4& v, float p, float (&acc)[8]) {
  const float pb = PB<I>::get(p);
#pragma unroll
  for (int k = 0; k < 4; ++k) {
    acc[2 * k] = fmaf(pb, bflo(v[k]), acc[2 * k]);
    acc[2 * k + 1] = fmaf(pb, bfhi(v[k]), acc[2 * k + 1]);
  }
}
template <int I, int N> struct PVLoop {
  __device__ static __forceinline__ void run(const u32x4 (&kv)[32], int base, float p, float (&acc)[8]) {
    pv_step<I>(kv[base + I], p, acc);
    PVLoop<I + 1, N>::run(kv, base, p, acc);
  }
};
template <int N> struct PVLoop<N, N> {
  __device__ static __forceinline__ void run(const u32x4 (&)[32], int, float, float (&)[8]) {}
};

__global__ void __launch_bounds__(NTHR, 2)
fused_kernel(const unsigned short* __restrict__ x_, const unsigned short* __restrict__ k_, const unsigned short* __restrict__ v_,
             const unsigned short* __restrict__ wq_, const unsigned short* __restrict__ wo_,
             const unsigned short* __restrict__ wg_, const unsigned short* __restrict__ wu_,
             const unsigned short* __restrict__ wd_, const unsigned short* __restrict__ gw_,
             unsigned short* __restrict__ out_, unsigned char* ws_) {
  __shared__ __attribute__((aligned(16))) float lds[8 * 128 + 8 * 128 + 8 * 128 + 8 * 128 + 64];
  float* const L_A = lds;
  float* const L_B = lds + 1024;
  float* const L_C = lds + 2048;
  float* const L_D = lds + 3072;
  float* const L_ML = lds + 4096;

  const int b = blockIdx.x, xcd = b & 7, jj = b >> 3;
  const int h = xcd * 4 + (jj & 3), s = jj >> 2;
  const int tid = threadIdx.x, lane = tid & 63, w = __builtin_amdgcn_readfirstlane(tid >> 6);
  const int g = lane >> 4, c = lane & 15, r8 = lane >> 3, c8 = lane & 7;
  const int nu = h < 12 ? 6 : 5, u0 = h < 12 ? 6 * h : 72 + 5 * (h - 12);
  const __amdgpu_buffer_rsrc_t rs = __builtin_amdgcn_make_buffer_rsrc((void*)ws_, 0, WS_BYTES, 0x00020000);

  const u32x2 ep = __builtin_amdgcn_raw_buffer_load_b64(rs, 0, WS_EPOCH, AUX_SC1);
  const gp4 xq = (gp4)(x_ + 64 * w + 16 * g);
  const u32x4 xf0 = xq[0], xf1 = xq[1];
  const u32x4 xg = ((gp4)x_)[lane], gwv = ((gp4)gw_)[lane];
  const int brow = 64 * s + 8 * w + r8;
  const unsigned xb = ((const GAS unsigned short*)x_)[brow];
  u32x4 wq[16];
  {
    const __amdgpu_buffer_rsrc_t rq = __builtin_amdgcn_make_buffer_rsrc((void*)(wq_ + (size_t)(64 * w) * (NH * DH) + h * DH), 0, 64 * NH * DH * 2, 0x00020000);
    const int voff = g * (16 * NH * DH * 2) + c * 16;
#pragma unroll
    for (int i = 0; i < 16; ++i) { wq[i] = __builtin_amdgcn_raw_buffer_load_b128(rq, voff, i * (NH * DH * 2), 0); SCHED_FENCE(); }
  }
  const int cu = u0 + (c8 < nu ? c8 : nu - 1);
  const u32x4 wgv = *(gp4)(wg_ + (size_t)brow * FF + cu * 8);
  const u32x4 wuv = *(gp4)(wu_ + (size_t)brow * FF + cu * 8);
  SCHED_FENCE();
  u32x4 kv[32];
  const size_t kvoff = ((size_t)h * SK + s * KEYS_WG + w * KEYS_WAVE) * DH;
  const __amdgpu_buffer_rsrc_t rk = __builtin_amdgcn_make_buffer_rsrc((void*)(k_ + kvoff), 0, KEYS_WAVE * DH * 2, 0x00020000);
  const __amdgpu_buffer_rsrc_t rv = __builtin_amdgcn_make_buffer_rsrc((void*)(v_ + kvoff), 0, KEYS_WAVE * DH * 2, 0x00020000);
#pragma unroll
  for (int i = 0; i < 32; ++i) { kv[i] = ld_kv(rk, lane * 16, i); SCHED_FENCE(); }

  float qa[8];
#pragma unroll
  for (int k = 0; k < 8; ++k) qa[k] = 0.f;
#pragma unroll
  for (int i = 0; i < 8; ++i) {
    const unsigned xp = i < 4 ? xf0[i & 3] : xf1[i & 3];
#pragma unroll
    for (int k = 0; k < 4; ++k) {
      const unsigned lo = __builtin_amdgcn_perm(wq[2 * i + 1][k], wq[2 * i][k], 0x05040100u);
      const unsigned hi = __builtin_amdgcn_perm(wq[2 * i + 1][k], wq[2 * i][k], 0x07060302u);
      qa[2 * k] = dot2(lo, xp, qa[2 * k]);
      qa[2 * k + 1] = dot2(hi, xp, qa[2 * k + 1]);
    }
  }
  {
    const float t0 = tr32(qa[0], qa[4]), t1 = tr32(qa[1], qa[5]), t2 = tr32(qa[2], qa[6]), t3 = tr32(qa[3], qa[7]);
    const float u0_ = tr16(t0, t2), u1_ = tr16(t1, t3);
    *(f32x2*)(L_A + w * 128 + 8 * c + 2 * g) = f32x2{u0_, u1_};
  }
  {
    const float xv = bflo(xb);
    float ga[8], ua[8];
#pragma unroll
    for (int k = 0; k < 4; ++k) {
      ga[2 * k] = xv * bflo(wgv[k]); ga[2 * k + 1] = xv * bfhi(wgv[k]);
      ua[2 * k] = xv * bflo(wuv[k]); ua[2 * k + 1] = xv * bfhi(wuv[k]);
    }
    float t[8];
#pragma unroll
    for (int k = 0; k < 8; ++k) t[k] = tr32(ga[k], ua[k]);
    float u[4];
#pragma unroll
    for (int k = 0; k < 4; ++k) u[k] = tr16(t[k], t[k + 4]);
    const float a0 = u[0] + dpp<XOR8>(u[0]), a1 = u[1] + dpp<XOR8>(u[1]), a2 = u[2] + dpp<XOR8>(u[2]), a3 = u[3] + dpp<XOR8>(u[3]);
    const bool b3 = (lane & 8) != 0;
    const int col = 8 * c8 + 4 * ((lane >> 4) & 1) + 2 * ((lane >> 3) & 1);
    *(f32x2*)(L_B + w * 128 + (lane >> 5) * 64 + col) = f32x2{b3 ? a2 : a0, b3 ? a3 : a1};
  }
  float gate, omg;
  {
    float z = 0.f;
#pragma unroll
    for (int k = 0; k < 4; ++k) {
      z += rbf(bflo(xg[k]) * bflo(gwv[k]));
      z += rbf(bfhi(xg[k]) * bfhi(gwv[k]));
    }
    z = rbf(xrow_sum(row_sum(z)));
    gate = rbf(1.0f / (1.0f + __expf(-z)));
    omg = rbf(1.0f - gate);
  }
  const unsigned tagA = __builtin_amdgcn_readfirstlane(ep[0]) + 1u, tagB = __builtin_amdgcn_readfirstlane(ep[1]) + 1u;
  __syncthreads();
  unsigned q[4];
  {
    float q0 = 0.f, q1 = 0.f;
#pragma unroll
    for (int ww = 0; ww < NWAVE; ++ww) { const f32x2 t = *(const f32x2*)(L_A + ww * 128 + 8 * c + 2 * g); q0 += t.x; q1 += t.y; }
    const unsigned qp = pack_bf16(q0, q1);
    auto r = __builtin_amdgcn_permlane32_swap(qp, qp, false, false);
    auto r0 = __builtin_amdgcn_permlane16_swap(r[0], r[0], false, false);
    auto r1 = __builtin_amdgcn_permlane16_swap(r[1], r[1], false, false);
    q[0] = r0[0]; q[1] = r0[1]; q[2] = r1[0]; q[3] = r1[1];
  }
  if (w == 7) {
    float gs = 0.f, us = 0.f;
#pragma unroll
    for (int ww = 0; ww < NWAVE; ++ww) { gs += L_B[ww * 128 + lane]; us += L_B[ww * 128 + 64 + lane]; }
    if (lane < 48) __builtin_amdgcn_raw_buffer_store_b128(u32x4{f2u(gs), tagA, f2u(us), tagB}, rs, lane * 16, WS_H0 + (h * 8 + s) * 1024, AUX_SC1);
  }

  float sc[2];
#pragma unroll
  for (int bt = 0; bt < 2; ++bt) {
    float xs[16];
#pragma unroll
    for (int i = 0; i < 16; ++i) {
      const u32x4 kk = kv[16 * bt + i];
      float d = dot2(kk[0], q[0], 0.f);
      d = dot2(kk[1], q[1], d); d = dot2(kk[2], q[2], d); d = dot2(kk[3], q[3], d);
      xs[i] = d;
      kv[16 * bt + i] = ld_kv(rv, lane * 16, 16 * bt + i);
      SCHED_FENCE();
    }
#define TR_STEP(HALF, CTRL, BIT)                                                        \
  _Pragma("unroll") for (int i = 0; i < (HALF); ++i) {                                  \
    const float lo_ = xs[i], hi_ = xs[i + (HALF)];                                      \
    const float t1_ = lo_ + dpp<CTRL>(lo_), t2_ = hi_ + dpp<CTRL>(hi_);                 \
    xs[i] = (c & (BIT)) ? t2_ : t1_;                                                    \
  }
    TR_STEP(8, XOR8, 8) TR_STEP(4, XOR7, 4) TR_STEP(2, XOR2, 2) TR_STEP(1, XOR1, 1)
#undef TR_STEP
    sc[bt] = rbf(rbf(xs[0]) * 0.088388347648318440f) * 1.4426950408889634f;
  }
  u32x4 wov[2];
  {
    const gp4 p = (gp4)(wo_ + (size_t)(h * DH + 16 * w + r8) * D + 64 * s + 8 * c8);
    wov[0] = p[0]; wov[1] = p[8 * D / 8];
  }
  const int wrow = (w < nu ? w : nu - 1);
  const u32x4 wdv = *(gp4)(wd_ + (size_t)(8 * u0 + 8 * wrow + r8) * D + 64 * s + 8 * c8);
  const int h0off = (lane < 48 ? lane : 47) * 16;
  u32x4 h0 = ld_gran(rs, h0off, WS_H0 + (h * 8 + w) * 1024);
  SCHED_FENCE();

  const float mw = xrow_max(row_max(fmaxf(sc[0], sc[1])));
  const float p0 = __builtin_amdgcn_exp2f(sc[0] - mw), p1 = __builtin_amdgcn_exp2f(sc[1] - mw);
  const float lw = xrow_sum(row_sum(p0 + p1));

  float acc[8];
#pragma unroll
  for (int k = 0; k < 8; ++k) acc[k] = 0.f;
  PVLoop<0, 16>::run(kv, 0, p0, acc);
  PVLoop<0, 16>::run(kv, 16, p1, acc);
  {
    const float t0 = tr32(acc[0], acc[4]), t1 = tr32(acc[1], acc[5]), t2 = tr32(acc[2], acc[6]), t3 = tr32(acc[3], acc[7]);
    const float u0_ = tr16(t0, t2), u1_ = tr16(t1, t3);
    *(f32x2*)(L_A + w * 128 + 8 * c + 2 * g) = f32x2{u0_, u1_};
    if (lane == 0) *(f32x2*)(L_ML + 2 * w) = f32x2{mw, lw};
  }
  for (unsigned spins = 0; !__all(h0[1] == tagA && h0[3] == tagB) && spins < SPIN_CAP; ++spins) {
    __builtin_amdgcn_s_sleep(2);
    h0 = ld_gran(rs, h0off, WS_H0 + (h * 8 + w) * 1024);
  }
  *(f32x2*)(L_C + w * 128 + 2 * lane) = f32x2{u2f(h0[0]), u2f(h0[2])};
  __syncthreads();

  if (w == 0) {
    float mm[8], ll[8];
#pragma unroll
    for (int ww = 0; ww < NWAVE; ++ww) { const f32x2 t = *(const f32x2*)(L_ML + 2 * ww); mm[ww] = t.x; ll[ww] = t.y; }
    float M = mm[0];
#pragma unroll
    for (int ww = 1; ww < NWAVE; ++ww) M = fmaxf(M, mm[ww]);
    float Ls = 0.f, o0 = 0.f, o1 = 0.f;
#pragma unroll
    for (int ww = 0; ww < NWAVE; ++ww) {
      const float wt = __builtin_amdgcn_exp2f(mm[ww] - M);
      const f32x2 t = *(const f32x2*)(L_A + ww * 128 + 2 * lane);
      Ls = fmaf(wt, ll[ww], Ls); o0 = fmaf(wt, t.x, o0); o1 = fmaf(wt, t.y, o1);
    }
    const int base = WS_H1 + (h * 8 + s) * 2048;
    __builtin_amdgcn_raw_buffer_store_b128(u32x4{f2u(o0), tagA, f2u(o1), tagB}, rs, lane * 16, base, AUX_SC1);
    if (lane == 0) __builtin_amdgcn_raw_buffer_store_b128(u32x4{f2u(M), tagA, f2u(Ls), tagB}, rs, 1024, base, AUX_SC1);
  }
  float ya[8], yb[8];
  {
    float gs = 0.f, us = 0.f;
    const int f = 8 * wrow + r8;
#pragma unroll
    for (int ww = 0; ww < NWAVE; ++ww) { const f32x2 t = *(const f32x2*)(L_C + ww * 128 + 2 * f); gs += t.x; us += t.y; }
    float hv = rbf(rbf(gs) * rbf(us));
    hv = w < nu ? hv : 0.f;
#pragma unroll
    for (int k = 0; k < 4; ++k) { yb[2 * k] = hv * bflo(wdv[k]); yb[2 * k + 1] = hv * bfhi(wdv[k]); }
  }
  {
    u32x4 ga, gm;
    const int base = WS_H1 + (h * 8) * 2048;
    unsigned spins = 0;
    for (;;) {
      ga = ld_gran(rs, lane * 16, base + w * 2048);
      gm = ld_gran(rs, c8 * 2048 + 1024, base);
      if (__all(ga[1] == tagA && ga[3] == tagB && gm[1] == tagA && gm[3] == tagB) || ++spins >= SPIN_CAP) break;
      __builtin_amdgcn_s_sleep(1);
    }
    const float ms = u2f(gm[0]), ls = u2f(gm[2]);
    float M = fmaxf(ms, dpp<XOR1>(ms)); M = fmaxf(M, dpp<XOR2>(M)); M = fmaxf(M, dpp<XOR7>(M));
    const float wt = __builtin_amdgcn_exp2f(ms - M);
    float Lt = wt * ls; Lt += dpp<XOR1>(Lt); Lt += dpp<XOR2>(Lt); Lt += dpp<XOR7>(Lt);
    const float sw = u2f(__builtin_amdgcn_readlane(f2u(wt / Lt), w));
    *(f32x2*)(L_B + w * 128 + 2 * lane) = f32x2{u2f(ga[0]) * sw, u2f(ga[2]) * sw};
  }
  __syncthreads();
  {
    float o0 = 0.f, o1 = 0.f;
#pragma unroll
    for (int ww = 0; ww < NWAVE; ++ww) { o0 += L_B[ww * 128 + 16 * w + r8]; o1 += L_B[ww * 128 + 16 * w + 8 + r8]; }
    o0 = rbf(o0); o1 = rbf(o1);
#pragma unroll
    for (int k = 0; k < 4; ++k) {
      ya[2 * k] = fmaf(o0, bflo(wov[0][k]), o1 * bflo(wov[1][k]));
      ya[2 * k + 1] = fmaf(o0, bfhi(wov[0][k]), o1 * bfhi(wov[1][k]));
    }
    float t[8];
#pragma unroll
    for (int k = 0; k < 8; ++k) t[k] = tr32(ya[k], yb[k]);
    float u[4];
#pragma unroll
    for (int k = 0; k < 4; ++k) u[k] = tr16(t[k], t[k + 4]);
    const float a0 = u[0] + dpp<XOR8>(u[0]), a1 = u[1] + dpp<XOR8>(u[1]), a2 = u[2] + dpp<XOR8>(u[2]), a3 = u[3] + dpp<XOR8>(u[3]);
    const bool b3 = (lane & 8) != 0;
    const int col = 8 * c8 + 4 * ((lane >> 4) & 1) + 2 * ((lane >> 3) & 1);
    *(f32x2*)(L_D + w * 128 + (lane >> 5) * 64 + col) = f32x2{b3 ? a2 : a0, b3 ? a3 : a1};
  }
  __syncthreads();
  if (w == 0) {
    float as = 0.f, bs = 0.f;
#pragma unroll
    for (int ww = 0; ww < NWAVE; ++ww) { as += L_D[ww * 128 + lane]; bs += L_D[ww * 128 + 64 + lane]; }
    __builtin_amdgcn_raw_buffer_store_b128(u32x4{f2u(as), tagA, f2u(bs), tagB}, rs, lane * 16, WS_H2 + (s * 32 + h) * 1024, AUX_SC1);
  } else if (w == 1 && s == (h >> 2)) {
    u32x4 gr[8];
    const int base = WS_H2 + (s * 32) * 1024 + (h & 3) * 256;
    const int voff = g * 1024 + c * 16;
    unsigned spins = 0;
    for (;;) {
      bool ok = true;
#pragma unroll
      for (int i = 0; i < 8; ++i) gr[i] = ld_gran(rs, voff, base + i * 4096);
#pragma unroll
      for (int i = 0; i < 8; ++i) ok = ok && gr[i][1] == tagA && gr[i][3] == tagB;
      if (__all(ok) || ++spins >= SPIN_CAP) break;
      __builtin_amdgcn_s_sleep(1);
    }
    float as = 0.f, bs = 0.f;
#pragma unroll
    for (int i = 0; i < 8; ++i) { as += u2f(gr[i][0]); bs += u2f(gr[i][2]); }
    as = rbf(xrow_sum(as)); bs = rbf(xrow_sum(bs));
    const float res = rbf(gate * as) + rbf(omg * bs);
    const unsigned short o16 = __builtin_bit_cast(unsigned short, (__bf16)res);
    if (lane < 16) out_[64 * s + 16 * (h & 3) + lane] = o16;
    if (h == 0 && lane == 0) __builtin_amdgcn_raw_buffer_store_b64(u32x2{tagA, tagB}, rs, 0, WS_EPOCH, AUX_SC1);
  }
}

extern "C" void kernel_launch(void* const* d_in, const int* in_sizes, int n_in, void* d_out, int out_size, void* d_ws,
                              size_t ws_size, hipStream_t stream) {
  (void)in_sizes; (void)n_in; (void)out_size; (void)ws_size;
  typedef const unsigned short* cp;
  fused_kernel<<<NWG, NTHR, 0, stream>>>((cp)d_in[0], (cp)d_in[1], (cp)d_in[2], (cp)d_in[3], (cp)d_in[4], (cp)d_in[6],
                                         (cp)d_in[7], (cp)d_in[8], (cp)d_in[9], (unsigned short*)d_out,
                                         (unsigned char*)d_ws);
}
```

```cpp
#define STAMPS 1
#define MAP 1
#define WIN 4
#include <hip/hip_runtime.h>
#include <hip/hip_bf16.h>
#include <stdint.h>

typedef unsigned u32x4 __attribute__((ext_vector_type(4)));
typedef unsigned u32x2 __attribute__((ext_vector_type(2)));
typedef float f32x2 __attribute__((ext_vector_type(2)));
typedef __bf16 bf16x2 __attribute__((ext_vector_type(2)));
#define GAS __attribute__((address_space(1)))
typedef const GAS u32x4* gp4;

constexpr int D = 512, NH = 32, SK = 8192, DH = 128, FF = 1376;
constexpr int NWAVE = 8, NTHR = 512, NWG = 256;
constexpr int KEYS_WG = 1024, KEYS_WAVE = 128;
constexpr int WS_EPOCH = 0;
constexpr int WS_HQ = 4096;
constexpr int WS_H0 = WS_HQ + NWG * 1024;
constexpr int WS_H1 = WS_H0 + NWG * 1024;
constexpr int WS_H2 = WS_H1 + NWG * 2048;
constexpr int WS_BYTES = WS_H2 + NWG * 1024;
constexpr int AUX_SC1 = 16, AUX_NT = 2;
constexpr unsigned SPIN_CAP = 1u << 18;

#define KVMODE 1
#define TAILPOS 4

__device__ __forceinline__ float dot2(unsigned a, unsigned b, float c) {
  return __builtin_amdgcn_fdot2_f32_bf16(__builtin_bit_cast(bf16x2, a), __builtin_bit_cast(bf16x2, b), c, false);
}
__device__ __forceinline__ float bflo(unsigned u) { return __builtin_bit_cast(float, u << 16); }
__device__ __forceinline__ float bfhi(unsigned u) { return __builtin_bit_cast(float, u & 0xffff0000u); }
__device__ __forceinline__ unsigned pack_bf16(float lo, float hi) {
  bf16x2 r; r.x = (__bf16)lo; r.y = (__bf16)hi; return __builtin_bit_cast(unsigned, r);
}
__device__ __forceinline__ float rbf(float x) { return (float)(__bf16)x; }
template <int CTRL> __device__ __forceinline__ float dpp(float x) {
  return __builtin_bit_cast(float, __builtin_amdgcn_mov_dpp(__builtin_bit_cast(int, x), CTRL, 0xf, 0xf, true));
}
constexpr int XOR1 = 0xB1, XOR2 = 0x4E, XOR7 = 0x141, XOR8 = 0x128;
__device__ __forceinline__ unsigned f2u(float x) { return __builtin_bit_cast(unsigned, x); }
__device__ __forceinline__ float u2f(unsigned x) { return __builtin_bit_cast(float, x); }
__device__ __forceinline__ float tr32(float a, float b) {
  auto r = __builtin_amdgcn_permlane32_swap(f2u(a), f2u(b), false, false);
  return u2f(r[0]) + u2f(r[1]);
}
__device__ __forceinline__ float tr16(float a, float b) {
  auto r = __builtin_amdgcn_permlane16_swap(f2u(a), f2u(b), false, false);
  return u2f(r[0]) + u2f(r[1]);
}
__device__ __forceinline__ float xrow_sum(float x) { x = tr16(x, x); return tr32(x, x); }
__device__ __forceinline__ float xrow_max(float x) {
  auto s = __builtin_amdgcn_permlane16_swap(f2u(x), f2u(x), false, false);
  x = fmaxf(u2f(s[0]), u2f(s[1]));
  auto t = __builtin_amdgcn_permlane32_swap(f2u(x), f2u(x), false, false);
  return fmaxf(u2f(t[0]), u2f(t[1]));
}
__device__ __forceinline__ float row_sum(float x) {
  x += dpp<XOR1>(x); x += dpp<XOR2>(x); x += dpp<XOR7>(x); x += dpp<XOR8>(x); return x;
}
__device__ __forceinline__ float row_max(float x) {
  x = fmaxf(x, dpp<XOR1>(x)); x = fmaxf(x, dpp<XOR2>(x)); x = fmaxf(x, dpp<XOR7>(x)); x = fmaxf(x, dpp<XOR8>(x)); return x;
}
__device__ __forceinline__ u32x4 ld_kv(__amdgpu_buffer_rsrc_t r, int voff, int i) {
  return __builtin_amdgcn_raw_buffer_load_b128(r, voff, i * 8192, AUX_NT);
}
__device__ __forceinline__ u32x4 ld_nt(gp4 p) { return __builtin_nontemporal_load(p); }
#define PACE(n) asm volatile("s_waitcnt vmcnt(%0)" ::"i"(n) : "memory")
__device__ __forceinline__ u32x4 ld_gran(__amdgpu_buffer_rsrc_t r, int voff, int soff) {
  asm volatile("" : "+v"(voff)::"memory");
  return __builtin_amdgcn_raw_buffer_load_b128(r, voff, soff, AUX_SC1);
}
__device__ __forceinline__ u32x4 repoll(__amdgpu_buffer_rsrc_t r, int voff, int soff) {
  u32x4 v;
  asm volatile("buffer_load_dwordx4 %0, %1, %2, %3 offen sc1\n\ts_waitcnt vmcnt(0)" : "=&v"(v) : "v"(voff), "s"(r), "s"(soff) : "memory");
  return v;
}
__device__ __forceinline__ bool tags_ok(const u32x4& v, unsigned tagA, unsigned tagB) { return __all((v[1] == tagA) & (v[3] == tagB)); }
__device__ __forceinline__ void st_gran(__amdgpu_buffer_rsrc_t r, int voff, int soff, float a, float b, unsigned tagA, unsigned tagB) {
  __builtin_amdgcn_raw_buffer_store_b128(u32x4{f2u(a), tagA, f2u(b), tagB}, r, voff, soff, AUX_SC1);
}
#define SCHED_FENCE() __builtin_amdgcn_sched_barrier(0)

constexpr int NST = 12;
#define STAMP(i) do { SCHED_FENCE(); st_[i] = __builtin_amdgcn_s_memrealtime(); SCHED_FENCE(); } while (0)
#define STAMP_DECL unsigned long long st_[NST]; for (int i_ = 0; i_ < NST; ++i_) st_[i_] = 0;
#define STAMP_OUT() do { if (lane == 0) { unsigned long long* sp_ = (unsigned long long*)(ws_ + (16 << 20)) + ((size_t)blockIdx.x * 8 + w) * 16; for (int i_ = 0; i_ < NST; ++i_) sp_[i_] = st_[i_]; sp_[15] = __builtin_amdgcn_s_getreg(20 | (3 << 11)) & 0xf; sp_[14] = dbg_spins; } } while (0)

template <int I> __device__ __forceinline__ void pv_step(const u32x4& v, float p, float (&acc)[8]) {
  const float pb = dpp<0x150 + I>(p);
#pragma unroll
  for (int k = 0; k < 4; ++k) {
    acc[2 * k] = fmaf(pb, bflo(v[k]), acc[2 * k]);
    acc[2 * k + 1] = fmaf(pb, bfhi(v[k]), acc[2 * k + 1]);
  }
  asm volatile("" : "+v"(acc[0]), "+v"(acc[1]), "+v"(acc[2]), "+v"(acc[3]), "+v"(acc[4]), "+v"(acc[5]), "+v"(acc[6]), "+v"(acc[7]));
}
template <int I, int N> struct PVLoop {
  template <class F, class G>
  __device__ static __forceinline__ void run(u32x4 (&kv)[64], __amdgpu_buffer_rsrc_t rv, int voff, float p0, float p1, float (&acc)[8], F&& tail, G&& mid) {
    constexpr int n = I + WIN;
    if constexpr (n < 32) { kv[32 + n] = ld_kv(rv, voff, n); SCHED_FENCE(); }
    if constexpr (I == TAILPOS) { tail(); SCHED_FENCE(); }
    if constexpr (I == TAILPOS + WIN + 1) { mid(); SCHED_FENCE(); }
    pv_step<I & 15>(kv[32 + I], I < 16 ? p0 : p1, acc);
    SCHED_FENCE();
    PVLoop<I + 1, N>::run(kv, rv, voff, p0, p1, acc, tail, mid);
  }
};
template <int N> struct PVLoop<N, N> {
  template <class F, class G>
  __device__ static __forceinline__ void run(u32x4 (&)[64], __amdgpu_buffer_rsrc_t, int, float, float, float (&)[8], F&&, G&&) {}
};

__global__ void __launch_bounds__(NTHR, 2)
fused_kernel(const unsigned short* __restrict__ x_, const unsigned short* __restrict__ k_, const unsigned short* __restrict__ v_,
             const unsigned short* __restrict__ wq_, const unsigned short* __restrict__ wo_,
             const unsigned short* __restrict__ wg_, const unsigned short* __restrict__ wu_,
             const unsigned short* __restrict__ wd_, const unsigned short* __restrict__ gw_,
             unsigned short* __restrict__ out_, unsigned char* ws_) {
  __shared__ __attribute__((aligned(16))) float lds[6 * 1024 + 64];
  float* const L_A = lds;
  float* const L_B = lds + 1024;
  float* const L_C = lds + 2048;
  float* const L_G = lds + 3072;
  float* const L_Y = lds + 4096;
  float* const L_ML = lds + 6144;

  const int b = blockIdx.x;
  const int h = (b & 7) * 4 + ((b >> 3) & 3), s = b >> 5;
  const int tid = threadIdx.x, lane = tid & 63, w = __builtin_amdgcn_readfirstlane(tid >> 6);
  const int g = lane >> 4, c = lane & 15, r8 = lane >> 3, c8 = lane & 7;
  const int nu = h < 12 ? 6 : 5, u0 = h < 12 ? 6 * h : 72 + 5 * (h - 12);
  const __amdgpu_buffer_rsrc_t rs = __builtin_amdgcn_make_buffer_rsrc((void*)ws_, 0, WS_BYTES, 0x00020000);
  unsigned dbg_spins = 0;
  asm volatile("" ::: "v135");

  STAMP_DECL
  STAMP(0);
  const int row0 = 64 * s + 8 * w;
  u32x4 wq0, wq1;
  {
    const gp4 p = (gp4)(wq_ + (size_t)(row0 + 2 * g) * (NH * DH) + h * DH + 8 * c);
    wq0 = ld_nt(p); wq1 = ld_nt(p + NH * DH / 8);
  }
  const unsigned xp = ((const GAS unsigned*)x_)[(row0 >> 1) + g];
  const unsigned xb = ((const GAS unsigned short*)x_)[row0 + r8];
  const u32x2 ep = __builtin_amdgcn_raw_buffer_load_b64(rs, 0, WS_EPOCH, AUX_SC1);
  const int cu = u0 + (c8 < nu ? c8 : nu - 1);
  const u32x4 wgv = ld_nt((gp4)(wg_ + (size_t)(row0 + r8) * FF + cu * 8));
  const u32x4 wuv = ld_nt((gp4)(wu_ + (size_t)(row0 + r8) * FF + cu * 8));
  SCHED_FENCE();
  u32x4 kv[64];
  const size_t kvoff = ((size_t)h * SK + s * KEYS_WG + w * 4) * DH;
  constexpr int KVBYTES = KEYS_WG * DH * 2 - 7 * 1024;
  const __amdgpu_buffer_rsrc_t rk = __builtin_amdgcn_make_buffer_rsrc((void*)(k_ + kvoff), 0, KVBYTES, 0x00020000);
  const __amdgpu_buffer_rsrc_t rv = __builtin_amdgcn_make_buffer_rsrc((void*)(v_ + kvoff), 0, KVBYTES, 0x00020000);
#pragma unroll
  for (int i = 0; i < WIN; ++i) { kv[i] = ld_kv(rk, lane * 16, i); SCHED_FENCE(); }

  {
    float qa[8];
#pragma unroll
    for (int k = 0; k < 4; ++k) {
      qa[2 * k] = dot2(__builtin_amdgcn_perm(wq1[k], wq0[k], 0x05040100u), xp, 0.f);
      qa[2 * k + 1] = dot2(__builtin_amdgcn_perm(wq1[k], wq0[k], 0x07060302u), xp, 0.f);
    }
    const float t0 = tr32(qa[0], qa[4]), t1 = tr32(qa[1], qa[5]), t2 = tr32(qa[2], qa[6]), t3 = tr32(qa[3], qa[7]);
    *(f32x2*)(L_A + w * 128 + 8 * c + 2 * g) = f32x2{tr16(t0, t2), tr16(t1, t3)};
  }
  {
    const float xv = bflo(xb);
    float t[8];
#pragma unroll
    for (int k = 0; k < 4; ++k) {
      t[2 * k] = tr32(xv * bflo(wgv[k]), xv * bflo(wuv[k]));
      t[2 * k + 1] = tr32(xv * bfhi(wgv[k]), xv * bfhi(wuv[k]));
    }
    float u[4];
#pragma unroll
    for (int k = 0; k < 4; ++k) u[k] = tr16(t[k], t[k + 4]);
    const float a0 = u[0] + dpp<XOR8>(u[0]), a1 = u[1] + dpp<XOR8>(u[1]), a2 = u[2] + dpp<XOR8>(u[2]), a3 = u[3] + dpp<XOR8>(u[3]);
    const bool b3 = (lane & 8) != 0;
    const int col = 8 * c8 + 4 * ((lane >> 4) & 1) + 2 * ((lane >> 3) & 1);
    *(f32x2*)(L_B + w * 128 + (lane >> 5) * 64 + col) = f32x2{b3 ? a2 : a0, b3 ? a3 : a1};
  }
  const unsigned tagA = __builtin_amdgcn_readfirstlane(ep[0]) + 1u, tagB = __builtin_amdgcn_readfirstlane(ep[1]) + 1u;
  STAMP(1);
  __syncthreads();
  if (w == 0) {
    float q0 = 0.f, q1 = 0.f;
#pragma unroll
    for (int ww = 0; ww < NWAVE; ++ww) { const f32x2 t = *(const f32x2*)(L_A + ww * 128 + 2 * lane); q0 += t.x; q1 += t.y; }
    st_gran(rs, lane * 16, WS_HQ + (h * 8 + s) * 1024, q0, q1, tagA, tagB);
  } else if (w == 7) {
    float gs = 0.f, us = 0.f;
#pragma unroll
    for (int ww = 0; ww < NWAVE; ++ww) { gs += L_B[ww * 128 + lane]; us += L_B[ww * 128 + 64 + lane]; }
    if (lane < 48) st_gran(rs, lane * 16, WS_H0 + (h * 8 + s) * 1024, gs, us, tagA, tagB);
  }
  SCHED_FENCE();
#pragma unroll
  for (int i = WIN; i < 16; ++i) { PACE(WIN - 1); kv[i] = ld_kv(rk, lane * 16, i); SCHED_FENCE(); }
  u32x4 hq = ld_gran(rs, lane * 16, WS_HQ + (h * 8 + w) * 1024);
  SCHED_FENCE();
#pragma unroll
  for (int i = 16; i < 16 + WIN; ++i) { PACE(WIN - 1); kv[i] = ld_kv(rk, lane * 16, i); SCHED_FENCE(); }
  STAMP(2);
  if (!tags_ok(hq, tagA, tagB)) {
    unsigned spins = 0;
    do { hq = repoll(rs, lane * 16, WS_HQ + (h * 8 + w) * 1024); ++dbg_spins; } while (!tags_ok(hq, tagA, tagB) && ++spins < SPIN_CAP);
  }
  *(f32x2*)(L_C + w * 128 + 2 * lane) = f32x2{u2f(hq[0]), u2f(hq[2])};
  __syncthreads();
  STAMP(3);
  unsigned q[4];
  {
    float q0 = 0.f, q1 = 0.f;
#pragma unroll
    for (int ww = 0; ww < NWAVE; ++ww) { const f32x2 t = *(const f32x2*)(L_C + ww * 128 + 8 * c + 2 * g); q0 += t.x; q1 += t.y; }
    const unsigned qp = pack_bf16(q0, q1);
    auto r = __builtin_amdgcn_permlane32_swap(qp, qp, false, false);
    auto r0 = __builtin_amdgcn_permlane16_swap(r[0], r[0], false, false);
    auto r1 = __builtin_amdgcn_permlane16_swap(r[1], r[1], false, false);
    q[0] = r0[0]; q[1] = r0[1]; q[2] = r1[0]; q[3] = r1[1];
  }

  float sc[2];
#pragma unroll
  for (int bt = 0; bt < 2; ++bt) {
    float xs[16];
#pragma unroll
    for (int i = 0; i < 16; ++i) {
      if (bt == 1) {
        const int n = 16 + i + WIN;
        kv[n] = ld_kv(n < 32 ? rk : rv, lane * 16, n & 31);
        SCHED_FENCE();
      }
      const u32x4 kk = kv[16 * bt + i];
      float d = dot2(kk[0], q[0], 0.f);
      d = dot2(kk[1], q[1], d); d = dot2(kk[2], q[2], d); d = dot2(kk[3], q[3], d);
      xs[i] = d;
      if (bt == 1) SCHED_FENCE();
    }
#define TR_STEP(HALF, CTRL, BIT)                                                        \
  _Pragma("unroll") for (int i = 0; i < (HALF); ++i) {                                  \
    const float lo_ = xs[i], hi_ = xs[i + (HALF)];                                      \
    const float t1_ = lo_ + dpp<CTRL>(lo_), t2_ = hi_ + dpp<CTRL>(hi_);                 \
    xs[i] = (c & (BIT)) ? t2_ : t1_;                                                    \
  }
    TR_STEP(8, XOR8, 8) TR_STEP(4, XOR7, 4) TR_STEP(2, XOR2, 2) TR_STEP(1, XOR1, 1)
#undef TR_STEP
    sc[bt] = rbf(rbf(xs[0]) * 0.088388347648318440f) * 1.4426950408889634f;
  }
  STAMP(4);
  const float mw = xrow_max(row_max(fmaxf(sc[0], sc[1])));
  const float p0 = __builtin_amdgcn_exp2f(sc[0] - mw), p1 = __builtin_amdgcn_exp2f(sc[1] - mw);
  const float lw = xrow_sum(row_sum(p0 + p1));
  if (lane == 0) *(f32x2*)(L_ML + 2 * w) = f32x2{mw, lw};

  u32x4 wov[2], wdv, h0, xg, gwv;
  const int wrow = (w < nu ? w : nu - 1);
  const int h0off = (lane < 48 ? lane : 47) * 16;
  auto tail_loads = [&]() {
    const gp4 p = (gp4)(wo_ + (size_t)(h * DH + 16 * w + 2 * c8) * D + 64 * s + 8 * r8);
    wov[0] = ld_nt(p); wov[1] = ld_nt(p + D / 8);
    wdv = ld_nt((gp4)(wd_ + (size_t)(8 * u0 + 8 * wrow + c8) * D + 64 * s + 8 * r8));
    xg = ((gp4)x_)[lane]; gwv = ((gp4)gw_)[lane];
    h0 = ld_gran(rs, h0off, WS_H0 + (h * 8 + w) * 1024);
  };
  float gate, omg, fscale, M_wg, L_wg;
  auto mid_work = [&]() {
    if (!tags_ok(h0, tagA, tagB)) {
      unsigned spins = 0;
      do { h0 = repoll(rs, h0off, WS_H0 + (h * 8 + w) * 1024); dbg_spins += 1000; } while (!tags_ok(h0, tagA, tagB) && ++spins < SPIN_CAP);
    }
    *(f32x2*)(L_G + w * 128 + 2 * lane) = f32x2{u2f(h0[0]), u2f(h0[2])};
    float z = 0.f;
#pragma unroll
    for (int k = 0; k < 4; ++k) {
      z += rbf(bflo(xg[k]) * bflo(gwv[k]));
      z += rbf(bfhi(xg[k]) * bfhi(gwv[k]));
    }
    z = rbf(xrow_sum(row_sum(z)));
    gate = rbf(1.0f / (1.0f + __expf(-z)));
    omg = rbf(1.0f - gate);
    __syncthreads();
    const f32x2 ml = *(const f32x2*)(L_ML + 2 * c8);
    float M = fmaxf(ml.x, dpp<XOR1>(ml.x)); M = fmaxf(M, dpp<XOR2>(M)); M = fmaxf(M, dpp<XOR7>(M));
    float Ls = __builtin_amdgcn_exp2f(ml.x - M) * ml.y; Ls += dpp<XOR1>(Ls); Ls += dpp<XOR2>(Ls); Ls += dpp<XOR7>(Ls);
    M_wg = M; L_wg = Ls;
    fscale = __builtin_amdgcn_exp2f(mw - M);
    if (w == 1 && lane == 0) st_gran(rs, 1024, WS_H1 + (h * 8 + s) * 2048, M, Ls, tagA, tagB);
    float gs = 0.f, us = 0.f;
    const int f = 8 * wrow + c8;
#pragma unroll
    for (int ww = 0; ww < NWAVE; ++ww) { const f32x2 t = *(const f32x2*)(L_G + ww * 128 + 2 * f); gs += t.x; us += t.y; }
    float hv = rbf(rbf(gs) * rbf(us));
    hv = w < nu ? hv : 0.f;
    float y[8];
#pragma unroll
    for (int k = 0; k < 4; ++k) { y[2 * k] = hv * bflo(wdv[k]); y[2 * k + 1] = hv * bfhi(wdv[k]); }
#define TR8(HALF, CTRL, BIT)                                                            \
  _Pragma("unroll") for (int i = 0; i < (HALF); ++i) {                                  \
    const float lo_ = y[i], hi_ = y[i + (HALF)];                                        \
    const float t1_ = lo_ + dpp<CTRL>(lo_), t2_ = hi_ + dpp<CTRL>(hi_);                 \
    y[i] = (c8 & (BIT)) ? t2_ : t1_;                                                    \
  }
    TR8(4, XOR7, 4) TR8(2, XOR2, 2) TR8(1, XOR1, 1)
    L_Y[512 + w * 64 + lane] = y[0];
  };

  float acc[8];
#pragma unroll
  for (int k = 0; k < 8; ++k) acc[k] = 0.f;
  PVLoop<0, 32>::run(kv, rv, lane * 16, p0, p1, acc, tail_loads, mid_work);
  {
    const float t0 = tr32(acc[0], acc[4]), t1 = tr32(acc[1], acc[5]), t2 = tr32(acc[2], acc[6]), t3 = tr32(acc[3], acc[7]);
    *(f32x2*)(L_A + w * 128 + 8 * c + 2 * g) = f32x2{tr16(t0, t2) * fscale, tr16(t1, t3) * fscale};
  }
  STAMP(5);
  __syncthreads();
  STAMP(6);
  if (w == 0) {
    float o0 = 0.f, o1 = 0.f;
#pragma unroll
    for (int ww = 0; ww < NWAVE; ++ww) { const f32x2 t = *(const f32x2*)(L_A + ww * 128 + 2 * lane); o0 += t.x; o1 += t.y; }
    st_gran(rs, lane * 16, WS_H1 + (h * 8 + s) * 2048, o0, o1, tagA, tagB);
  }
  {
    u32x4 ga, gm;
    const int base = WS_H1 + (h * 8) * 2048;
    unsigned spins = 0;
    for (;;) {
      ga = ld_gran(rs, r8 * 2048 + c8 * 16, base + w * 128);
      gm = ld_gran(rs, r8 * 2048 + 1024, base);
      if (__all(ga[1] == tagA && ga[3] == tagB && gm[1] == tagA && gm[3] == tagB) || ++spins >= SPIN_CAP) break;
    }
    STAMP(7);
    const float ms = u2f(gm[0]), ls = u2f(gm[2]);
    const float M = xrow_max(fmaxf(ms, dpp<XOR8>(ms)));
    const float wt = __builtin_amdgcn_exp2f(ms - M);
    float Lt = wt * ls, o0 = wt * u2f(ga[0]), o1 = wt * u2f(ga[2]);
    Lt += dpp<XOR8>(Lt); o0 += dpp<XOR8>(o0); o1 += dpp<XOR8>(o1);
    Lt = xrow_sum(Lt); o0 = xrow_sum(o0); o1 = xrow_sum(o1);
    const float inv = 1.0f / Lt;
    o0 = rbf(o0 * inv); o1 = rbf(o1 * inv);
    float y[8];
#pragma unroll
    for (int k = 0; k < 4; ++k) {
      y[2 * k] = fmaf(o0, bflo(wov[0][k]), o1 * bflo(wov[1][k]));
      y[2 * k + 1] = fmaf(o0, bfhi(wov[0][k]), o1 * bfhi(wov[1][k]));
    }
    TR8(4, XOR7, 4) TR8(2, XOR2, 2) TR8(1, XOR1, 1)
#undef TR8
    L_Y[w * 64 + lane] = y[0];
  }
  STAMP(8);
  __syncthreads();
  STAMP(9);
  if (w == 0) {
    float as = 0.f, bs = 0.f;
#pragma unroll
    for (int ww = 0; ww < NWAVE; ++ww) { as += L_Y[ww * 64 + lane]; bs += L_Y[512 + ww * 64 + lane]; }
    st_gran(rs, lane * 16, WS_H2 + (s * 32 + h) * 1024, as, bs, tagA, tagB);
  } else if (w == 1 && s == (h >> 2)) {
    u32x4 gr[8];
    const int base = WS_H2 + (s * 32) * 1024 + (h & 3) * 256;
    const int voff = g * 1024 + c * 16;
    unsigned spins = 0;
    for (;;) {
      bool ok = true;
#pragma unroll
      for (int i = 0; i < 8; ++i) gr[i] = ld_gran(rs, voff, base + i * 4096);
#pragma unroll
      for (int i = 0; i < 8; ++i) ok = ok & (gr[i][1] == tagA) & (gr[i][3] == tagB);
      if (__all(ok) || ++spins >= SPIN_CAP) break;
    }
    STAMP(10);
    float as = 0.f, bs = 0.f;
#pragma unroll
    for (int i = 0; i < 8; ++i) { as += u2f(gr[i][0]); bs += u2f(gr[i][2]); }
    as = rbf(xrow_sum(as)); bs = rbf(xrow_sum(bs));
    const float res = rbf(gate * as) + rbf(omg * bs);
    const unsigned short o16 = __builtin_bit_cast(unsigned short, (__bf16)res);
    if (lane < 16) out_[64 * s + 16 * (h & 3) + lane] = o16;
    if (h == 0 && lane == 0) __builtin_amdgcn_raw_buffer_store_b64(u32x2{tagA, tagB}, rs, 0, WS_EPOCH, AUX_SC1);
  }
  STAMP(11);
  STAMP_OUT();
}

#include <vector>
#include <algorithm>
#include <cstdio>
static void dump_stamps(void* ws, int it) {
  std::vector<unsigned long long> h(NWG * 8 * 16);
  (void)hipMemcpy(h.data(), (char*)ws + (16 << 20), h.size() * 8, hipMemcpyDeviceToHost);
  unsigned long long t0 = ~0ull;
  for (int i = 0; i < NWG * 8; ++i) t0 = std::min(t0, h[i * 16]);
  printf("stamps it=%d (us from first entry): idx min / p10 / med / p90 / max\n", it);
  for (int k = 0; k < NST; ++k) {
    std::vector<double> v;
    for (int i = 0; i < NWG * 8; ++i) if (h[i * 16 + k]) v.push_back((h[i * 16 + k] - t0) * 0.01);
    if (v.empty()) continue;
    std::sort(v.begin(), v.end());
    printf("  %2d: %6.2f %6.2f %6.2f %6.2f %6.2f  (n=%zu)\n", k, v[0], v[v.size() / 10], v[v.size() / 2], v[v.size() * 9 / 10], v.back(), v.size());
  }
  double mx[8] = {0}, mn[8]; for (int i = 0; i < 8; ++i) mn[i] = 1e9;
  int bad = 0; unsigned long long sp = 0, spw = 0;
  for (int i = 0; i < NWG * 8; ++i) { int x = h[i * 16 + 15] & 7; double t = (h[i * 16 + 5] - t0) * 0.01; mx[x] = std::max(mx[x], t); mn[x] = std::min(mn[x], t); if (x != (h[15] + i / 8) % 8) ++bad; sp += h[i * 16 + 14]; spw += h[i * 16 + 14] != 0; }
  printf("  V-end per XCD min/max:"); for (int i = 0; i < 8; ++i) printf(" %.1f/%.1f", mn[i], mx[i]); printf("  (xcd0=%d, off-pattern waves %d) early spins %llu in %llu waves\n", (int)h[15], bad, sp, spw);
}

__global__ void diag_fill(uint4* p, size_t n, unsigned v) {
  size_t i = blockIdx.x * (size_t)blockDim.x + threadIdx.x;
  for (; i < n; i += (size_t)gridDim.x * blockDim.x) { unsigned x = (unsigned)(i * 2654435761u + v); p[i] = make_uint4(x, x ^ 0x1234567u, x * 3u, x + 77u); }
}
extern "C" void kernel_launch(void* const* d_in, const int* in_sizes, int n_in, void* d_out, int out_size, void* d_ws,
                              size_t ws_size, hipStream_t stream) {
  typedef const unsigned short* cp;
  auto go = [&]() {
  fused_kernel<<<NWG, NTHR, 0, stream>>>((cp)d_in[0], (cp)d_in[1], (cp)d_in[2], (cp)d_in[3], (cp)d_in[4], (cp)d_in[6],
                                         (cp)d_in[7], (cp)d_in[8], (cp)d_in[9], (unsigned short*)d_out,
                                         (unsigned char*)d_ws); };
  go();
  hipStreamCaptureStatus cs = hipStreamCaptureStatusNone;
  (void)hipStreamIsCapturing(stream, &cs);
  if (cs == hipStreamCaptureStatusNone) {
    (void)hipStreamSynchronize(stream);
    for (int i = 0; i < n_in; ++i) { hipPointerAttribute_t a; (void)hipPointerGetAttributes(&a, d_in[i]); printf("in[%d]=%p n=%d type=%d managed=%d flags=%u\n", i, d_in[i], in_sizes[i], (int)a.type, a.isManaged, a.allocationFlags); }
    printf("out=%p ws=%p ws_size=%zu\n", d_out, d_ws, ws_size);
    printf("=== first direct call\n"); dump_stamps(d_ws, 0);
    for (int r = 0; r < 3; ++r) { go(); (void)hipStreamSynchronize(stream); }
    printf("=== warm\n"); dump_stamps(d_ws, 1);
    for (int r = 0; r < 3; ++r) {
      diag_fill<<<2048, 256, 0, stream>>>((uint4*)d_ws, ws_size / 16, r + 5);
      (void)hipStreamSynchronize(stream);
      go(); (void)hipStreamSynchronize(stream);
      printf("=== after own ws fill\n"); dump_stamps(d_ws, 2 + r);
    }
    fflush(stdout);
  }
}
```

```cpp
#define NOPIN 1
#include <hip/hip_runtime.h>
#include <hip/hip_bf16.h>
#include <stdint.h>

typedef unsigned u32x4 __attribute__((ext_vector_type(4)));
typedef unsigned u32x2 __attribute__((ext_vector_type(2)));
typedef float f32x2 __attribute__((ext_vector_type(2)));
typedef __bf16 bf16x2 __attribute__((ext_vector_type(2)));
#define GAS __attribute__((address_space(1)))
typedef const GAS u32x4* gp4;

constexpr int D = 512, NH = 32, SK = 8192, DH = 128, FF = 1376;
constexpr int NWAVE = 8, NTHR = 512, NWG = 256;
constexpr int KEYS_WG = 1024, KEYS_WAVE = 128;
constexpr int WS_EPOCH = 0;
constexpr int WS_HQ = 4096;
constexpr int WS_H0 = WS_HQ + NWG * 1024;
constexpr int WS_H1 = WS_H0 + NWG * 1024;
constexpr int WS_H2 = WS_H1 + NWG * 2048;
constexpr int WS_BYTES = WS_H2 + NWG * 1024;
constexpr int AUX_SC1 = 16, AUX_NT = 2;
constexpr unsigned SPIN_CAP = 1u << 18;

#define KVMODE 1
#define WIN 4
#define TAILPOS 4
#define MAP 1
#define KAUX 2
#define VAUX 2
#define WNT 1

__device__ __forceinline__ float dot2(unsigned a, unsigned b, float c) {
  return __builtin_amdgcn_fdot2_f32_bf16(__builtin_bit_cast(bf16x2, a), __builtin_bit_cast(bf16x2, b), c, false);
}
__device__ __forceinline__ float bflo(unsigned u) { return __builtin_bit_cast(float, u << 16); }
__device__ __forceinline__ float bfhi(unsigned u) { return __builtin_bit_cast(float, u & 0xffff0000u); }
__device__ __forceinline__ unsigned pack_bf16(float lo, float hi) {
  bf16x2 r; r.x = (__bf16)lo; r.y = (__bf16)hi; return __builtin_bit_cast(unsigned, r);
}
__device__ __forceinline__ float rbf(float x) { return (float)(__bf16)x; }
template <int CTRL> __device__ __forceinline__ float dpp(float x) {
  return __builtin_bit_cast(float, __builtin_amdgcn_mov_dpp(__builtin_bit_cast(int, x), CTRL, 0xf, 0xf, true));
}
constexpr int XOR1 = 0xB1, XOR2 = 0x4E, XOR7 = 0x141, XOR8 = 0x128;
__device__ __forceinline__ unsigned f2u(float x) { return __builtin_bit_cast(unsigned, x); }
__device__ __forceinline__ float u2f(unsigned x) { return __builtin_bit_cast(float, x); }
__device__ __forceinline__ float tr32(float a, float b) {
  auto r = __builtin_amdgcn_permlane32_swap(f2u(a), f2u(b), false, false);
  return u2f(r[0]) + u2f(r[1]);
}
__device__ __forceinline__ float tr16(float a, float b) {
  auto r = __builtin_amdgcn_permlane16_swap(f2u(a), f2u(b), false, false);
  return u2f(r[0]) + u2f(r[1]);
}
__device__ __forceinline__ float xrow_sum(float x) { x = tr16(x, x); return tr32(x, x); }
__device__ __forceinline__ float xrow_max(float x) {
  auto s = __builtin_amdgcn_permlane16_swap(f2u(x), f2u(x), false, false);
  x = fmaxf(u2f(s[0]), u2f(s[1]));
  auto t = __builtin_amdgcn_permlane32_swap(f2u(x), f2u(x), false, false);
  return fmaxf(u2f(t[0]), u2f(t[1]));
}
__device__ __forceinline__ float row_sum(float x) {
  x += dpp<XOR1>(x); x += dpp<XOR2>(x); x += dpp<XOR7>(x); x += dpp<XOR8>(x); return x;
}
__device__ __forceinline__ float row_max(float x) {
  x = fmaxf(x, dpp<XOR1>(x)); x = fmaxf(x, dpp<XOR2>(x)); x = fmaxf(x, dpp<XOR7>(x)); x = fmaxf(x, dpp<XOR8>(x)); return x;
}
template <int AUX> __device__ __forceinline__ u32x4 ld_kv(__amdgpu_buffer_rsrc_t r, int voff, int i) {
  return __builtin_amdgcn_raw_buffer_load_b128(r, voff, i * 8192, AUX);
}
__device__ __forceinline__ u32x4 ld_nt(gp4 p) { return __builtin_nontemporal_load(p); }
#define PACE(n) asm volatile("s_waitcnt vmcnt(%0)" ::"i"(n) : "memory")
__device__ __forceinline__ u32x4 ld_gran(__amdgpu_buffer_rsrc_t r, int voff, int soff) {
  asm volatile("" : "+v"(voff)::"memory");
  return __builtin_amdgcn_raw_buffer_load_b128(r, voff, soff, AUX_SC1);
}
__device__ __forceinline__ u32x4 repoll(__amdgpu_buffer_rsrc_t r, int voff, int soff) {
  u32x4 v;
  asm volatile("buffer_load_dwordx4 %0, %1, %2, %3 offen sc1\n\ts_waitcnt vmcnt(0)" : "=&v"(v) : "v"(voff), "s"(r), "s"(soff) : "memory");
  return v;
}
__device__ __forceinline__ bool tags_ok(const u32x4& v, unsigned tagA, unsigned tagB) { return __all((v[1] == tagA) & (v[3] == tagB)); }
__device__ __forceinline__ void st_gran(__amdgpu_buffer_rsrc_t r, int voff, int soff, float a, float b, unsigned tagA, unsigned tagB) {
  __builtin_amdgcn_raw_buffer_store_b128(u32x4{f2u(a), tagA, f2u(b), tagB}, r, voff, soff, AUX_SC1);
}
#define SCHED_FENCE() __builtin_amdgcn_sched_barrier(0)

#define STAMP(i)
#define STAMP_DECL
#define STAMP_OUT()

template <int I> __device__ __forceinline__ void pv_step(const u32x4& v, float p, float (&acc)[8]) {
  const float pb = dpp<0x150 + I>(p);
#pragma unroll
  for (int k = 0; k < 4; ++k) {
    acc[2 * k] = fmaf(pb, bflo(v[k]), acc[2 * k]);
    acc[2 * k + 1] = fmaf(pb, bfhi(v[k]), acc[2 * k + 1]);
  }
  asm volatile("" : "+v"(acc[0]), "+v"(acc[1]), "+v"(acc[2]), "+v"(acc[3]), "+v"(acc[4]), "+v"(acc[5]), "+v"(acc[6]), "+v"(acc[7]));
}
template <int I, int N> struct PVLoop {
  template <class F, class G>
  __device__ static __forceinline__ void run(u32x4 (&kv)[64], __amdgpu_buffer_rsrc_t rv, int voff, float p0, float p1, float (&acc)[8], F&& tail, G&& mid) {
    constexpr int n = I + WIN;
    if constexpr (n < 32) { kv[32 + n] = ld_kv<VAUX>(rv, voff, n); SCHED_FENCE(); }
    if constexpr (I == TAILPOS) { tail(); SCHED_FENCE(); }
    if constexpr (I == TAILPOS + WIN + 1) { mid(); SCHED_FENCE(); }
    pv_step<I & 15>(kv[32 + I], I < 16 ? p0 : p1, acc);
    SCHED_FENCE();
    PVLoop<I + 1, N>::run(kv, rv, voff, p0, p1, acc, tail, mid);
  }
};
template <int N> struct PVLoop<N, N> {
  template <class F, class G>
  __device__ static __forceinline__ void run(u32x4 (&)[64], __amdgpu_buffer_rsrc_t, int, float, float, float (&)[8], F&&, G&&) {}
};

__global__ void __launch_bounds__(NTHR, 2)
fused_kernel(const unsigned short* __restrict__ x_, const unsigned short* __restrict__ k_, const unsigned short* __restrict__ v_,
             const unsigned short* __restrict__ wq_, const unsigned short* __restrict__ wo_,
             const unsigned short* __restrict__ wg_, const unsigned short* __restrict__ wu_,
             const unsigned short* __restrict__ wd_, const unsigned short* __restrict__ gw_,
             unsigned short* __restrict__ out_, unsigned char* ws_) {
  __shared__ __attribute__((aligned(16))) float lds[6 * 1024 + 64];
  float* const L_A = lds;
  float* const L_B = lds + 1024;
  float* const L_C = lds + 2048;
  float* const L_G = lds + 3072;
  float* const L_Y = lds + 4096;
  float* const L_ML = lds + 6144;

  const int b = blockIdx.x;
  const int h = (b & 7) * 4 + ((b >> 3) & 3), s = b >> 5;
  const int tid = threadIdx.x, lane = tid & 63, w = __builtin_amdgcn_readfirstlane(tid >> 6);
  const int g = lane >> 4, c = lane & 15, r8 = lane >> 3, c8 = lane & 7;
  const int nu = h < 12 ? 6 : 5, u0 = h < 12 ? 6 * h : 72 + 5 * (h - 12);
  const __amdgpu_buffer_rsrc_t rs = __builtin_amdgcn_make_buffer_rsrc((void*)ws_, 0, WS_BYTES, 0x00020000);
  unsigned dbg_spins = 0;

  STAMP_DECL
  STAMP(0);
  const int row0 = 64 * s + 8 * w;
  u32x4 wq0, wq1;
  {
    const gp4 p = (gp4)(wq_ + (size_t)(row0 + 2 * g) * (NH * DH) + h * DH + 8 * c);
    wq0 = ld_nt(p); wq1 = ld_nt(p + NH * DH / 8);
  }
  const unsigned xp = ((const GAS unsigned*)x_)[(row0 >> 1) + g];
  const unsigned xb = ((const GAS unsigned short*)x_)[row0 + r8];
  const u32x2 ep = __builtin_amdgcn_raw_buffer_load_b64(rs, 0, WS_EPOCH, AUX_SC1);
  const int cu = u0 + (c8 < nu ? c8 : nu - 1);
  const u32x4 wgv = ld_nt((gp4)(wg_ + (size_t)(row0 + r8) * FF + cu * 8));
  const u32x4 wuv = ld_nt((gp4)(wu_ + (size_t)(row0 + r8) * FF + cu * 8));
  SCHED_FENCE();
  u32x4 kv[64];
  const size_t kvoff = ((size_t)h * SK + s * KEYS_WG + w * 4) * DH;
  constexpr int KVBYTES = KEYS_WG * DH * 2 - 7 * 1024;
  const __amdgpu_buffer_rsrc_t rk = __builtin_amdgcn_make_buffer_rsrc((void*)(k_ + kvoff), 0, KVBYTES, 0x00020000);
  const __amdgpu_buffer_rsrc_t rv = __builtin_amdgcn_make_buffer_rsrc((void*)(v_ + kvoff), 0, KVBYTES, 0x00020000);
#pragma unroll
  for (int i = 0; i < WIN; ++i) { kv[i] = ld_kv<KAUX>(rk, lane * 16, i); SCHED_FENCE(); }

  {
    float qa[8];
#pragma unroll
    for (int k = 0; k < 4; ++k) {
      qa[2 * k] = dot2(__builtin_amdgcn_perm(wq1[k], wq0[k], 0x05040100u), xp, 0.f);
      qa[2 * k + 1] = dot2(__builtin_amdgcn_perm(wq1[k], wq0[k], 0x07060302u), xp, 0.f);
    }
    const float t0 = tr32(qa[0], qa[4]), t1 = tr32(qa[1], qa[5]), t2 = tr32(qa[2], qa[6]), t3 = tr32(qa[3], qa[7]);
    *(f32x2*)(L_A + w * 128 + 8 * c + 2 * g) = f32x2{tr16(t0, t2), tr16(t1, t3)};
  }
  {
    const float xv = bflo(xb);
    float t[8];
#pragma unroll
    for (int k = 0; k < 4; ++k) {
      t[2 * k] = tr32(xv * bflo(wgv[k]), xv * bflo(wuv[k]));
      t[2 * k + 1] = tr32(xv * bfhi(wgv[k]), xv * bfhi(wuv[k]));
    }
    float u[4];
#pragma unroll
    for (int k = 0; k < 4; ++k) u[k] = tr16(t[k], t[k + 4]);
    const float a0 = u[0] + dpp<XOR8>(u[0]), a1 = u[1] + dpp<XOR8>(u[1]), a2 = u[2] + dpp<XOR8>(u[2]), a3 = u[3] + dpp<XOR8>(u[3]);
    const bool b3 = (lane & 8) != 0;
    const int col = 8 * c8 + 4 * ((lane >> 4) & 1) + 2 * ((lane >> 3) & 1);
    *(f32x2*)(L_B + w * 128 + (lane >> 5) * 64 + col) = f32x2{b3 ? a2 : a0, b3 ? a3 : a1};
  }
  const unsigned tagA = __builtin_amdgcn_readfirstlane(ep[0]) + 1u, tagB = __builtin_amdgcn_readfirstlane(ep[1]) + 1u;
  STAMP(1);
  __syncthreads();
  if (w == 0) {
    float q0 = 0.f, q1 = 0.f;
#pragma unroll
    for (int ww = 0; ww < NWAVE; ++ww) { const f32x2 t = *(const f32x2*)(L_A + ww * 128 + 2 * lane); q0 += t.x; q1 += t.y; }
    st_gran(rs, lane * 16, WS_HQ + (h * 8 + s) * 1024, q0, q1, tagA, tagB);
  } else if (w == 7) {
    float gs = 0.f, us = 0.f;
#pragma unroll
    for (int ww = 0; ww < NWAVE; ++ww) { gs += L_B[ww * 128 + lane]; us += L_B[ww * 128 + 64 + lane]; }
    if (lane < 48) st_gran(rs, lane * 16, WS_H0 + (h * 8 + s) * 1024, gs, us, tagA, tagB);
  }
  SCHED_FENCE();
#pragma unroll
  for (int i = WIN; i < 16; ++i) { PACE(WIN - 1); kv[i] = ld_kv<KAUX>(rk, lane * 16, i); SCHED_FENCE(); }
  u32x4 hq = ld_gran(rs, lane * 16, WS_HQ + (h * 8 + w) * 1024);
  SCHED_FENCE();
#pragma unroll
  for (int i = 16; i < 16 + WIN; ++i) { PACE(WIN - 1); kv[i] = ld_kv<KAUX>(rk, lane * 16, i); SCHED_FENCE(); }
  STAMP(2);
  if (!tags_ok(hq, tagA, tagB)) {
    unsigned spins = 0;
    do { hq = repoll(rs, lane * 16, WS_HQ + (h * 8 + w) * 1024); ++dbg_spins; } while (!tags_ok(hq, tagA, tagB) && ++spins < SPIN_CAP);
  }
  *(f32x2*)(L_C + w * 128 + 2 * lane) = f32x2{u2f(hq[0]), u2f(hq[2])};
  __syncthreads();
  STAMP(3);
  unsigned q[4];
  {
    float q0 = 0.f, q1 = 0.f;
#pragma unroll
    for (int ww = 0; ww < NWAVE; ++ww) { const f32x2 t = *(const f32x2*)(L_C + ww * 128 + 8 * c + 2 * g); q0 += t.x; q1 += t.y; }
    const unsigned qp = pack_bf16(q0, q1);
    auto r = __builtin_amdgcn_permlane32_swap(qp, qp, false, false);
    auto r0 = __builtin_amdgcn_permlane16_swap(r[0], r[0], false, false);
    auto r1 = __builtin_amdgcn_permlane16_swap(r[1], r[1], false, false);
    q[0] = r0[0]; q[1] = r0[1]; q[2] = r1[0]; q[3] = r1[1];
  }

  float sc[2];
#pragma unroll
  for (int bt = 0; bt < 2; ++bt) {
    float xs[16];
#pragma unroll
    for (int i = 0; i < 16; ++i) {
      if (bt == 1) {
        const int n = 16 + i + WIN;
        kv[n] = n < 32 ? ld_kv<KAUX>(rk, lane * 16, n & 31) : ld_kv<VAUX>(rv, lane * 16, n & 31);
        SCHED_FENCE();
      }
      const u32x4 kk = kv[16 * bt + i];
      float d = dot2(kk[0], q[0], 0.f);
      d = dot2(kk[1], q[1], d); d = dot2(kk[2], q[2], d); d = dot2(kk[3], q[3], d);
      xs[i] = d;
      if (bt == 1) SCHED_FENCE();
    }
#define TR_STEP(HALF, CTRL, BIT)                                                        \
  _Pragma("unroll") for (int i = 0; i < (HALF); ++i) {                                  \
    const float lo_ = xs[i], hi_ = xs[i + (HALF)];                                      \
    const float t1_ = lo_ + dpp<CTRL>(lo_), t2_ = hi_ + dpp<CTRL>(hi_);                 \
    xs[i] = (c & (BIT)) ? t2_ : t1_;                                                    \
  }
    TR_STEP(8, XOR8, 8) TR_STEP(4, XOR7, 4) TR_STEP(2, XOR2, 2) TR_STEP(1, XOR1, 1)
#undef TR_STEP
    sc[bt] = rbf(rbf(xs[0]) * 0.088388347648318440f) * 1.4426950408889634f;
  }
  STAMP(4);
  const float mw = xrow_max(row_max(fmaxf(sc[0], sc[1])));
  const float p0 = __builtin_amdgcn_exp2f(sc[0] - mw), p1 = __builtin_amdgcn_exp2f(sc[1] - mw);
  const float lw = xrow_sum(row_sum(p0 + p1));
  if (lane == 0) *(f32x2*)(L_ML + 2 * w) = f32x2{mw, lw};

  u32x4 wov[2], wdv, h0, xg, gwv;
  const int wrow = (w < nu ? w : nu - 1);
  const int h0off = (lane < 48 ? lane : 47) * 16;
  auto tail_loads = [&]() {
    const gp4 p = (gp4)(wo_ + (size_t)(h * DH + 16 * w + 2 * c8) * D + 64 * s + 8 * r8);
    wov[0] = ld_nt(p); wov[1] = ld_nt(p + D / 8);
    wdv = ld_nt((gp4)(wd_ + (size_t)(8 * u0 + 8 * wrow + c8) * D + 64 * s + 8 * r8));
    xg = ((gp4)x_)[lane]; gwv = ((gp4)gw_)[lane];
    h0 = ld_gran(rs, h0off, WS_H0 + (h * 8 + w) * 1024);
  };
  float gate, omg, fscale, M_wg, L_wg;
  auto mid_work = [&]() {
    if (!tags_ok(h0, tagA, tagB)) {
      unsigned spins = 0;
      do { h0 = repoll(rs, h0off, WS_H0 + (h * 8 + w) * 1024); dbg_spins += 1000; } while (!tags_ok(h0, tagA, tagB) && ++spins < SPIN_CAP);
    }
    *(f32x2*)(L_G + w * 128 + 2 * lane) = f32x2{u2f(h0[0]), u2f(h0[2])};
    float z = 0.f;
#pragma unroll
    for (int k = 0; k < 4; ++k) {
      z += rbf(bflo(xg[k]) * bflo(gwv[k]));
      z += rbf(bfhi(xg[k]) * bfhi(gwv[k]));
    }
    z = rbf(xrow_sum(row_sum(z)));
    gate = rbf(1.0f / (1.0f + __expf(-z)));
    omg = rbf(1.0f - gate);
    __syncthreads();
    const f32x2 ml = *(const f32x2*)(L_ML + 2 * c8);
    float M = fmaxf(ml.x, dpp<XOR1>(ml.x)); M = fmaxf(M, dpp<XOR2>(M)); M = fmaxf(M, dpp<XOR7>(M));
    float Ls = __builtin_amdgcn_exp2f(ml.x - M) * ml.y; Ls += dpp<XOR1>(Ls); Ls += dpp<XOR2>(Ls); Ls += dpp<XOR7>(Ls);
    M_wg = M; L_wg = Ls;
    fscale = __builtin_amdgcn_exp2f(mw - M);
    if (w == 1 && lane == 0) st_gran(rs, 1024, WS_H1 + (h * 8 + s) * 2048, M, Ls, tagA, tagB);
    float gs = 0.f, us = 0.f;
    const int f = 8 * wrow + c8;
#pragma unroll
    for (int ww = 0; ww < NWAVE; ++ww) { const f32x2 t = *(const f32x2*)(L_G + ww * 128 + 2 * f); gs += t.x; us += t.y; }
    float hv = rbf(rbf(gs) * rbf(us));
    hv = w < nu ? hv : 0.f;
    float y[8];
#pragma unroll
    for (int k = 0; k < 4; ++k) { y[2 * k] = hv * bflo(wdv[k]); y[2 * k + 1] = hv * bfhi(wdv[k]); }
#define TR8(HALF, CTRL, BIT)                                                            \
  _Pragma("unroll") for (int i = 0; i < (HALF); ++i) {                                  \
    const float lo_ = y[i], hi_ = y[i + (HALF)];                                        \
    const float t1_ = lo_ + dpp<CTRL>(lo_), t2_ = hi_ + dpp<CTRL>(hi_);                 \
    y[i] = (c8 & (BIT)) ? t2_ : t1_;                                                    \
  }
    TR8(4, XOR7, 4) TR8(2, XOR2, 2) TR8(1, XOR1, 1)
    L_Y[512 + w * 64 + lane] = y[0];
  };

  float acc[8];
#pragma unroll
  for (int k = 0; k < 8; ++k) acc[k] = 0.f;
  PVLoop<0, 32>::run(kv, rv, lane * 16, p0, p1, acc, tail_loads, mid_work);
  {
    const float t0 = tr32(acc[0], acc[4]), t1 = tr32(acc[1], acc[5]), t2 = tr32(acc[2], acc[6]), t3 = tr32(acc[3], acc[7]);
    *(f32x2*)(L_A + w * 128 + 8 * c + 2 * g) = f32x2{tr16(t0, t2) * fscale, tr16(t1, t3) * fscale};
  }
  STAMP(5);
  __syncthreads();
  STAMP(6);
  if (w == 0) {
    float o0 = 0.f, o1 = 0.f;
#pragma unroll
    for (int ww = 0; ww < NWAVE; ++ww) { const f32x2 t = *(const f32x2*)(L_A + ww * 128 + 2 * lane); o0 += t.x; o1 += t.y; }
    st_gran(rs, lane * 16, WS_H1 + (h * 8 + s) * 2048, o0, o1, tagA, tagB);
  }
  {
    u32x4 ga, gm;
    const int base = WS_H1 + (h * 8) * 2048;
    unsigned spins = 0;
    for (;;) {
      ga = ld_gran(rs, r8 * 2048 + c8 * 16, base + w * 128);
      gm = ld_gran(rs, r8 * 2048 + 1024, base);
      if (__all(ga[1] == tagA && ga[3] == tagB && gm[1] == tagA && gm[3] == tagB) || ++spins >= SPIN_CAP) break;
    }
    STAMP(7);
    const float ms = u2f(gm[0]), ls = u2f(gm[2]);
    const float M = xrow_max(fmaxf(ms, dpp<XOR8>(ms)));
    const float wt = __builtin_amdgcn_exp2f(ms - M);
    float Lt = wt * ls, o0 = wt * u2f(ga[0]), o1 = wt * u2f(ga[2]);
    Lt += dpp<XOR8>(Lt); o0 += dpp<XOR8>(o0); o1 += dpp<XOR8>(o1);
    Lt = xrow_sum(Lt); o0 = xrow_sum(o0); o1 = xrow_sum(o1);
    const float inv = 1.0f / Lt;
    o0 = rbf(o0 * inv); o1 = rbf(o1 * inv);
    float y[8];
#pragma unroll
    for (int k = 0; k < 4; ++k) {
      y[2 * k] = fmaf(o0, bflo(wov[0][k]), o1 * bflo(wov[1][k]));
      y[2 * k + 1] = fmaf(o0, bfhi(wov[0][k]), o1 * bfhi(wov[1][k]));
    }
    TR8(4, XOR7, 4) TR8(2, XOR2, 2) TR8(1, XOR1, 1)
#undef TR8
    L_Y[w * 64 + lane] = y[0];
  }
  STAMP(8);
  __syncthreads();
  STAMP(9);
  if (w == 0) {
    float as = 0.f, bs = 0.f;
#pragma unroll
    for (int ww = 0; ww < NWAVE; ++ww) { as += L_Y[ww * 64 + lane]; bs += L_Y[512 + ww * 64 + lane]; }
    st_gran(rs, lane * 16, WS_H2 + (s * 32 + h) * 1024, as, bs, tagA, tagB);
  } else if (w == 1 && s == (h >> 2)) {
    u32x4 gr[8];
    const int base = WS_H2 + (s * 32) * 1024 + (h & 3) * 256;
    const int voff = g * 1024 + c * 16;
    unsigned spins = 0;
    for (;;) {
      bool ok = true;
#pragma unroll
      for (int i = 0; i < 8; ++i) gr[i] = ld_gran(rs, voff, base + i * 4096);
#pragma unroll
      for (int i = 0; i < 8; ++i) ok = ok & (gr[i][1] == tagA) & (gr[i][3] == tagB);
      if (__all(ok) || ++spins >= SPIN_CAP) break;
    }
    STAMP(10);
    float as = 0.f, bs = 0.f;
#pragma unroll
    for (int i = 0; i < 8; ++i) { as += u2f(gr[i][0]); bs += u2f(gr[i][2]); }
    as = rbf(xrow_sum(as)); bs = rbf(xrow_sum(bs));
    const float res = rbf(gate * as) + rbf(omg * bs);
    const unsigned short o16 = __builtin_bit_cast(unsigned short, (__bf16)res);
    if (lane < 16) out_[64 * s + 16 * (h & 3) + lane] = o16;
    if (h == 0 && lane == 0) __builtin_amdgcn_raw_buffer_store_b64(u32x2{tagA, tagB}, rs, 0, WS_EPOCH, AUX_SC1);
  }
  STAMP(11);
  STAMP_OUT();
}

extern "C" void kernel_launch(void* const* d_in, const int* in_sizes, int n_in, void* d_out, int out_size, void* d_ws,
                              size_t ws_size, hipStream_t stream) {
  (void)in_sizes; (void)n_in; (void)out_size; (void)ws_size;
  typedef const unsigned short* cp;
  fused_kernel<<<NWG, NTHR, 0, stream>>>((cp)d_in[0], (cp)d_in[1], (cp)d_in[2], (cp)d_in[3], (cp)d_in[4], (cp)d_in[6],
                                         (cp)d_in[7], (cp)d_in[8], (cp)d_in[9], (unsigned short*)d_out,
                                         (unsigned char*)d_ws);
}
```

```cpp
#include <hip/hip_runtime.h>
#include <hip/hip_bf16.h>
#include <stdint.h>

typedef unsigned u32x4 __attribute__((ext_vector_type(4)));
typedef unsigned u32x2 __attribute__((ext_vector_type(2)));
typedef float f32x2 __attribute__((ext_vector_type(2)));
typedef __bf16 bf16x2 __attribute__((ext_vector_type(2)));
#define GAS __attribute__((address_space(1)))
#define LAS __attribute__((address_space(3)))
typedef const GAS u32x4* gp4;

constexpr int D = 512, NH = 32, SK = 8192, DH = 128, FF = 1376;
constexpr int NTHR = 320, NWG = 256;
constexpr int KEYS_WG = 1024;
constexpr int WS_PR = 0;
constexpr int WS_H1 = WS_PR + 4 * 16384;
constexpr int WS_H2 = WS_H1 + NH * 16384;
constexpr int WS_HQ = WS_H2 + 32 * 16384;
constexpr int WS_HG = WS_HQ + NWG * 1024;
constexpr int WS_ML = WS_HG + NWG * 1024;
constexpr int WS_EPOCH = WS_ML + NWG * 128;
constexpr int WS_BYTES = WS_EPOCH + 128;
static_assert(WS_BYTES <= (2 << 20), "one 2 MiB page");
constexpr int WS_OOB = 0x04000000;
constexpr int AUX_SC1 = 16, AUX_NT = 2;
constexpr unsigned SPIN_CAP = 1u << 18;
constexpr unsigned TAG_A = 0x5bd1e995u, TAG_B = 0xc2b2ae35u;
constexpr int L_FLAG = 0;
constexpr int L_ML = 40;
constexpr int L_HALF = 48;
constexpr int L_P0 = 64;
constexpr int L_P1 = L_P0 + 512;
constexpr int L_P2 = L_P1 + 512;
constexpr int L_P3 = L_P2 + 512;
constexpr int L_OP = L_P3 + 512;
constexpr int L_Y = L_OP + 512;
constexpr int L_END = L_Y + 512;

constexpr int WIN = 8;
constexpr int NBACK = 56;
constexpr int CR = 8;
constexpr int IG = 30;
constexpr int IQ = 38;
constexpr int NEAR_THR = 110;
constexpr int VOTES = 3;

__device__ __forceinline__ float dot2(unsigned a, unsigned b, float c) {
  return __builtin_amdgcn_fdot2_f32_bf16(__builtin_bit_cast(bf16x2, a), __builtin_bit_cast(bf16x2, b), c, false);
}
__device__ __forceinline__ float bflo(unsigned u) { return __builtin_bit_cast(float, u << 16); }
__device__ __forceinline__ float bfhi(unsigned u) { return __builtin_bit_cast(float, u & 0xffff0000u); }
__device__ __forceinline__ unsigned pack_bf16(float lo, float hi) {
  bf16x2 r; r.x = (__bf16)lo; r.y = (__bf16)hi; return __builtin_bit_cast(unsigned, r);
}
__device__ __forceinline__ float rbf(float x) { return (float)(__bf16)x; }
__device__ __forceinline__ unsigned pair_lo(unsigned r0, unsigned r1) { return __builtin_amdgcn_perm(r1, r0, 0x05040100u); }
__device__ __forceinline__ unsigned pair_hi(unsigned r0, unsigned r1) { return __builtin_amdgcn_perm(r1, r0, 0x07060302u); }
template <int CTRL> __device__ __forceinline__ float dpp(float x) {
  return __builtin_bit_cast(float, __builtin_amdgcn_mov_dpp(__builtin_bit_cast(int, x), CTRL, 0xf, 0xf, true));
}
constexpr int XOR1 = 0xB1, XOR2 = 0x4E, XOR7 = 0x141, XOR8 = 0x128, ROR4 = 0x124;
__device__ __forceinline__ unsigned f2u(float x) { return __builtin_bit_cast(unsigned, x); }
__device__ __forceinline__ float u2f(unsigned x) { return __builtin_bit_cast(float, x); }
__device__ __forceinline__ float tr32(float a, float b) {
  auto r = __builtin_amdgcn_permlane32_swap(f2u(a), f2u(b), false, false);
  return u2f(r[0]) + u2f(r[1]);
}
__device__ __forceinline__ float tr16(float a, float b) {
  auto r = __builtin_amdgcn_permlane16_swap(f2u(a), f2u(b), false, false);
  return u2f(r[0]) + u2f(r[1]);
}
__device__ __forceinline__ float xrow_sum(float x) { x = tr16(x, x); return tr32(x, x); }
__device__ __forceinline__ float xrow_max(float x) {
  auto s = __builtin_amdgcn_permlane16_swap(f2u(x), f2u(x), false, false);
  x = fmaxf(u2f(s[0]), u2f(s[1]));
  auto t = __builtin_amdgcn_permlane32_swap(f2u(x), f2u(x), false, false);
  return fmaxf(u2f(t[0]), u2f(t[1]));
}
__device__ __forceinline__ float row_sum(float x) {
  x += dpp<XOR1>(x); x += dpp<XOR2>(x); x += dpp<XOR7>(x); x += dpp<XOR8>(x); return x;
}
__device__ __forceinline__ float row_max(float x) {
  x = fmaxf(x, dpp<XOR1>(x)); x = fmaxf(x, dpp<XOR2>(x)); x = fmaxf(x, dpp<XOR7>(x)); x = fmaxf(x, dpp<XOR8>(x)); return x;
}
template <int CTRL> __device__ __forceinline__ float trd(float lo, float hi, bool sel) {
  const float t1 = lo + dpp<CTRL>(lo), t2 = hi + dpp<CTRL>(hi);
  return sel ? t2 : t1;
}
__device__ __forceinline__ float tr8(const float (&y)[8], int c8) {
  float a[4], b[2];
#pragma unroll
  for (int i = 0; i < 4; ++i) a[i] = trd<XOR7>(y[i], y[i + 4], (c8 & 4) != 0);
#pragma unroll
  for (int i = 0; i < 2; ++i) b[i] = trd<XOR2>(a[i], a[i + 2], (c8 & 2) != 0);
  return trd<XOR1>(b[0], b[1], (c8 & 1) != 0);
}
__device__ __forceinline__ u32x4 ld_nt(gp4 p) { return __builtin_nontemporal_load(p); }
__device__ __forceinline__ u32x4 ld_kv(__amdgpu_buffer_rsrc_t r, int voff, int soff) {
  return __builtin_amdgcn_raw_buffer_load_b128(r, voff, soff, AUX_NT);
}
#define PACE(n) asm volatile("s_waitcnt vmcnt(%0)" ::"i"(n) : "memory")
#define LDS_DRAIN() asm volatile("s_waitcnt lgkmcnt(0)" ::: "memory")
#define SCHED_FENCE() __builtin_amdgcn_sched_barrier(0)
__device__ __forceinline__ u32x4 ld_gran(__amdgpu_buffer_rsrc_t r, int voff, int soff) {
  asm volatile("" : "+v"(voff)::"memory");
  return __builtin_amdgcn_raw_buffer_load_b128(r, voff, soff, AUX_SC1);
}
__device__ __forceinline__ void st_gran(__amdgpu_buffer_rsrc_t r, int voff, int soff, float a, float b, unsigned tagA, unsigned tagB) {
  __builtin_amdgcn_raw_buffer_store_b128(u32x4{f2u(a), tagA, f2u(b), tagB}, r, voff, soff, AUX_SC1);
}
template <int N, int STRIDE, int SLEEP>
__device__ __forceinline__ void gather(u32x4 (&gr)[N], __amdgpu_buffer_rsrc_t r, int voff, int base, unsigned tagA, unsigned tagB) {
  static_assert(N == 1 || N == 2, "");
  unsigned long long m[N];
#pragma unroll
  for (int i = 0; i < N; ++i) { m[i] = ~0ull; gr[i] = u32x4{0u, 0u, 0u, 0u}; }
#pragma unroll 1
  for (unsigned spins = 0;; ++spins) {
    if constexpr (N == 1)
      asm volatile("s_mov_b64 exec, %1\n\tbuffer_load_dwordx4 %0, %2, %3, %4 offen sc1\n\ts_mov_b64 exec, -1\n\ts_waitcnt vmcnt(0)"
                   : "+v"(gr[0]) : "s"(m[0]), "v"(voff), "s"(r), "s"(base) : "memory");
    else
      asm volatile("s_mov_b64 exec, %2\n\ts_cbranch_execz 1f\n\tbuffer_load_dwordx4 %0, %4, %5, %6 offen sc1\n1:\n\t"
                   "s_mov_b64 exec, %3\n\ts_cbranch_execz 2f\n\tbuffer_load_dwordx4 %1, %4, %5, %7 offen sc1\n2:\n\t"
                   "s_mov_b64 exec, -1\n\ts_waitcnt vmcnt(0)"
                   : "+v"(gr[0]), "+v"(gr[N - 1]) : "s"(m[0]), "s"(m[N - 1]), "v"(voff), "s"(r), "s"(base), "s"(base + STRIDE) : "memory");
    unsigned long long missing = 0ull;
#pragma unroll
    for (int i = 0; i < N; ++i) {
      m[i] = __builtin_amdgcn_ballot_w64((gr[i][1] != tagA) | (gr[i][3] != tagB));
      missing |= m[i];
    }
    if (missing == 0ull || spins >= SPIN_CAP) break;
    if (SLEEP) __builtin_amdgcn_s_sleep(SLEEP);
  }
}
template <int N, int STRIDE>
__device__ __forceinline__ void gather_finish(u32x4 (&gr)[N], __amdgpu_buffer_rsrc_t r, int voff, int base, unsigned tagA, unsigned tagB) {
  bool bad = false;
#pragma unroll
  for (int i = 0; i < N; ++i) bad = bad | (gr[i][1] != tagA) | (gr[i][3] != tagB);
  if (__builtin_amdgcn_ballot_w64(bad) != 0ull) gather<N, STRIDE, 2>(gr, r, voff, base, tagA, tagB);
}
typedef volatile LAS unsigned long long* lflag;
__device__ __forceinline__ bool lds_all4(lflag grp, int lane, unsigned tagA, unsigned tagB) {
  const unsigned long long f = grp[lane & 3];
  const bool ok = __builtin_amdgcn_ballot_w64(((unsigned)f != tagA) | ((unsigned)(f >> 32) != tagB)) == 0ull;
  asm volatile("" ::: "memory");
  return ok;
}
template <int SLEEP = 1>
__device__ __forceinline__ void lds_wait4(lflag grp, int lane, unsigned tagA, unsigned tagB) {
#pragma unroll 1
  for (unsigned spins = 0; !lds_all4(grp, lane, tagA, tagB) && spins < (SPIN_CAP << 4); ++spins) __builtin_amdgcn_s_sleep(SLEEP);
}
__device__ __forceinline__ void lds_post(lflag grp, int sw, int lane, unsigned tagA, unsigned tagB) {
  LDS_DRAIN();
  if (lane == 0) grp[sw] = ((unsigned long long)tagB << 32) | tagA;
}

__device__ __forceinline__ float qk_dot(const u32x4& kk, const u32x4& q) {
  float d = dot2(kk[0], q[0], 0.f);
  d = dot2(kk[1], q[1], d); d = dot2(kk[2], q[2], d); return dot2(kk[3], q[3], d);
}
__device__ __forceinline__ float score16(const float (&xs)[16], int c) {
  float a[8], b[4], d[2];
#pragma unroll
  for (int i = 0; i < 8; ++i) a[i] = trd<XOR8>(xs[i], xs[i + 8], (c & 8) != 0);
#pragma unroll
  for (int i = 0; i < 4; ++i) b[i] = trd<XOR7>(a[i], a[i + 4], (c & 4) != 0);
#pragma unroll
  for (int i = 0; i < 2; ++i) d[i] = trd<XOR2>(b[i], b[i + 2], (c & 2) != 0);
  const float s = trd<XOR1>(d[0], d[1], (c & 1) != 0);
  return rbf(rbf(s) * 0.088388347648318440f) * 1.4426950408889634f;
}
template <int I> __device__ __forceinline__ void pv_step(const u32x4& v, float p, float (&acc)[8]) {
  const float pb = dpp<0x150 + I>(p);
#pragma unroll
  for (int k = 0; k < 4; ++k) {
    acc[2 * k] = fmaf(pb, bflo(v[k]), acc[2 * k]);
    acc[2 * k + 1] = fmaf(pb, bfhi(v[k]), acc[2 * k + 1]);
  }
  asm volatile("" : "+v"(acc[0]), "+v"(acc[1]), "+v"(acc[2]), "+v"(acc[3]), "+v"(acc[4]), "+v"(acc[5]), "+v"(acc[6]), "+v"(acc[7]));
}
constexpr int RING = 8;
static_assert(WIN == RING, "a consumed slot is refilled in place");
template <int I, int E> struct PVS {
  __device__ static __forceinline__ void run(u32x4 (&ring)[RING], __amdgpu_buffer_rsrc_t rv, int voff, int soff, float p, float (&acc)[8]) {
    const u32x4 cur = ring[I % RING];
    ring[(I + WIN) % RING] = ld_kv(rv, voff, soff + I * 4096);
    SCHED_FENCE();
    pv_step<I>(cur, p, acc);
    SCHED_FENCE();
    PVS<I + 1, E>::run(ring, rv, voff, soff, p, acc);
  }
};
template <int E> struct PVS<E, E> {
  __device__ static __forceinline__ void run(u32x4 (&)[RING], __amdgpu_buffer_rsrc_t, int, int, float, float (&)[8]) {}
};

template <int FROM, int TO>
__device__ __forceinline__ void k_rows(u32x4 (&kb)[64], __amdgpu_buffer_rsrc_t rk, int voff) {
#pragma unroll
  for (int i = FROM; i < TO; ++i) { PACE(WIN - 1); kb[i] = ld_kv(rk, voff, i * 4096); SCHED_FENCE(); }
}

struct Params {
  const unsigned short *x, *k, *v, *wq, *wo, *wg, *wu, *wd, *gw;
  unsigned short* out;
  unsigned char* ws;
};
static_assert(sizeof(Params) == 88, "no padding");

__global__ void __launch_bounds__(NTHR, 2) fused_kernel(Params prm) {
  const unsigned short* __restrict__ x_ = prm.x; const unsigned short* __restrict__ k_ = prm.k; const unsigned short* __restrict__ v_ = prm.v;
  const unsigned short* __restrict__ wq_ = prm.wq; const unsigned short* __restrict__ wo_ = prm.wo; const unsigned short* __restrict__ wg_ = prm.wg;
  const unsigned short* __restrict__ wu_ = prm.wu; const unsigned short* __restrict__ wd_ = prm.wd; const unsigned short* __restrict__ gw_ = prm.gw;
  GAS unsigned short* out_ = (GAS unsigned short*)prm.out; unsigned char* ws_ = prm.ws;
  __shared__ __attribute__((aligned(16))) unsigned lds_raw[L_END];
  LAS float* const ldf = (LAS float*)lds_raw;
  const lflag flag = (lflag)((LAS unsigned*)lds_raw + L_FLAG);

  asm volatile("" ::"s"(x_), "s"(k_), "s"(v_), "s"(wq_), "s"(wo_), "s"(wg_), "s"(wu_), "s"(wd_), "s"(gw_), "s"(out_), "s"(ws_));
  const int b = blockIdx.x;
  const int h = (b & 7) * 4 + ((b >> 3) & 3), s = b >> 5;
  const int tid = threadIdx.x, lane = tid & 63, sw = __builtin_amdgcn_readfirstlane(tid >> 6) - 1;
  const int g = lane >> 4, c = lane & 15, r8 = lane >> 3, c8 = lane & 7;
  const int nu = h < 12 ? 6 : 5, u0 = h < 12 ? 6 * h : 72 + 5 * (h - 12);
  unsigned char* const ws2 = (unsigned char*)(((unsigned long long)ws_ + ((2ull << 20) - 1)) & ~((2ull << 20) - 1));
  const __amdgpu_buffer_rsrc_t rs = __builtin_amdgcn_make_buffer_rsrc((void*)ws2, 0, WS_BYTES, 0x00020000);
  LAS int* const ldi = (LAS int*)lds_raw;
  int vote = 0;
  if (sw < 0) {
    unsigned long long ta, tb;
    unsigned p0, p1;
    asm volatile("buffer_load_dword %2, off, %4, %5 sc1\n\tbuffer_load_dword %3, off, %4, %6 sc1\n\t"
                 "s_waitcnt vmcnt(1)\n\ts_memtime %0\n\ts_waitcnt vmcnt(0) lgkmcnt(0)\n\ts_memtime %1\n\ts_waitcnt lgkmcnt(0)"
                 : "=&s"(ta), "=&s"(tb), "=&v"(p0), "=&v"(p1)
                 : "s"(rs), "s"(WS_PR + (b >> 6) * 16384 + (b & 63) * 128), "s"(WS_PR + 8192 + (b >> 6) * 16384 + (b & 63) * 128) : "memory");
    vote = (unsigned)tb - (unsigned)ta > (unsigned)NEAR_THR;
  }
  int role = sw;
  asm volatile("" : "+s"(role));
  if (role < 0) {
    const u32x2 ep = __builtin_amdgcn_raw_buffer_load_b64(rs, 0, WS_EPOCH, AUX_SC1);
    const unsigned tagA = (__builtin_amdgcn_readfirstlane(ep[0]) + 1u) ^ TAG_A, tagB = (__builtin_amdgcn_readfirstlane(ep[1]) + 1u) ^ TAG_B;
    auto sum4 = [&](int base) {
      f32x2 t = *(const LAS f32x2*)(ldf + base + 2 * lane);
#pragma unroll
      for (int ww = 1; ww < 4; ++ww) t += *(const LAS f32x2*)(ldf + base + ww * 128 + 2 * lane);
      return t;
    };
    lds_wait4<2>(flag, lane, tagA, tagB);
    { const f32x2 t = sum4(L_P0); st_gran(rs, lane * 16, WS_HQ + (h * 8 + s) * 1024, t.x, t.y, tagA, tagB); }
    lds_wait4<8>(flag + 4, lane, tagA, tagB);
    { const f32x2 t = sum4(L_P1); st_gran(rs, lane < 8 * nu ? lane * 16 : WS_OOB, WS_HG + (h * 8 + s) * 1024, t.x, t.y, tagA, tagB); }
    lds_wait4<8>(flag + 12, lane, tagA, tagB);
    {
      const f32x2 ml = *(const LAS f32x2*)(ldf + L_ML + 2 * (lane & 3));
      float M = fmaxf(ml.x, dpp<XOR1>(ml.x)); M = fmaxf(M, dpp<XOR2>(M));
      float L = __builtin_amdgcn_exp2f(ml.x - M) * ml.y; L += dpp<XOR1>(L); L += dpp<XOR2>(L);
      st_gran(rs, lane == 0 ? 0 : WS_OOB, WS_ML + (h * 8 + s) * 128, M, vote ? -L : L, tagA, tagB);
    }
    __syncthreads();
    const int half = __builtin_amdgcn_readfirstlane(ldi[L_HALF]) * 8192;
    { const f32x2 t = sum4(L_OP); st_gran(rs, lane * 16, WS_H1 + h * 16384 + half + s * 1024, t.x, t.y, tagA, tagB); }
    __syncthreads();
    const float a = (ldf[L_Y + lane] + ldf[L_Y + 64 + lane]) + (ldf[L_Y + 128 + lane] + ldf[L_Y + 192 + lane]);
    const float bb = (ldf[L_Y + 256 + lane] + ldf[L_Y + 320 + lane]) + (ldf[L_Y + 384 + lane] + ldf[L_Y + 448 + lane]);
    const int o2 = WS_H2 + (s * 4 + (h >> 3)) * 16384 + (h & 7) * 1024;
    st_gran(rs, lane * 16, o2 + (8192 - half), a, bb, tagA, tagB);
    st_gran(rs, lane * 16, o2 + half, a, bb, tagA, tagB);
    return;
  }

  const size_t kvoff = ((size_t)h * SK + s * KEYS_WG + sw * 4) * DH;
  constexpr int KVBYTES = KEYS_WG * DH * 2 - 3 * 1024;
  const __amdgpu_buffer_rsrc_t rk = __builtin_amdgcn_make_buffer_rsrc((void*)(k_ + kvoff), 0, KVBYTES, 0x00020000);
  const __amdgpu_buffer_rsrc_t rv = __builtin_amdgcn_make_buffer_rsrc((void*)(v_ + kvoff), 0, KVBYTES, 0x00020000);
  const int voff = lane * 16;
  const int row0 = 64 * s + 16 * sw;

  u32x4 kb[64], ring[RING], wa[4], gq[2];
  {
    const __amdgpu_buffer_rsrc_t rq = __builtin_amdgcn_make_buffer_rsrc((void*)(wq_ + (size_t)row0 * (NH * DH) + h * DH), 0, 16 * NH * DH * 2, 0x00020000);
#pragma unroll
    for (int i = 0; i < 4; ++i) { wa[i] = ld_kv(rq, g * (4 * NH * DH * 2) + c * 16, i * (NH * DH * 2)); SCHED_FENCE(); }
  }
  const u32x2 xq = *(const GAS u32x2*)(x_ + row0 + 4 * g);
  const unsigned xr = *(const GAS unsigned*)(x_ + row0 + 2 * r8);
  const u32x2 ep = __builtin_amdgcn_raw_buffer_load_b64(rs, 0, WS_EPOCH, AUX_SC1);
  SCHED_FENCE();
#pragma unroll
  for (int i = 0; i < WIN; ++i) { kb[i] = ld_kv(rk, voff, i * 4096); SCHED_FENCE(); }
  const unsigned epA = __builtin_amdgcn_readfirstlane(ep[0]) + 1u, epB = __builtin_amdgcn_readfirstlane(ep[1]) + 1u;
  const unsigned tagA = epA ^ TAG_A, tagB = epB ^ TAG_B;
  {
    float qa[8];
#pragma unroll
    for (int k = 0; k < 4; ++k) {
      qa[2 * k] = dot2(pair_lo(wa[2][k], wa[3][k]), xq[1], dot2(pair_lo(wa[0][k], wa[1][k]), xq[0], 0.f));
      qa[2 * k + 1] = dot2(pair_hi(wa[2][k], wa[3][k]), xq[1], dot2(pair_hi(wa[0][k], wa[1][k]), xq[0], 0.f));
    }
    const float t0 = tr32(qa[0], qa[4]), t1 = tr32(qa[1], qa[5]), t2 = tr32(qa[2], qa[6]), t3 = tr32(qa[3], qa[7]);
    *(LAS f32x2*)(ldf + L_P0 + sw * 128 + 8 * c + 2 * g) = f32x2{tr16(t0, t2), tr16(t1, t3)};
    lds_post(flag, sw, lane, tagA, tagB);
  }
  SCHED_FENCE();
  static_assert(IG + WIN <= IQ && IQ + WIN < NBACK, "order of the side steps");
  k_rows<WIN, IG + 1>(kb, rk, voff);
  {
    const size_t o = (size_t)(row0 + 2 * r8) * FF + (u0 + (c8 < nu ? c8 : nu - 1)) * 8;
    wa[0] = ld_nt((gp4)(wg_ + o)); wa[1] = ld_nt((gp4)(wg_ + o + FF)); wa[2] = ld_nt((gp4)(wu_ + o)); wa[3] = ld_nt((gp4)(wu_ + o + FF));
    SCHED_FENCE();
  }
  k_rows<IG + 1, IG + WIN + 1>(kb, rk, voff);
  {
    float ga[8], ua[8];
#pragma unroll
    for (int k = 0; k < 4; ++k) {
      ga[2 * k] = dot2(pair_lo(wa[0][k], wa[1][k]), xr, 0.f); ga[2 * k + 1] = dot2(pair_hi(wa[0][k], wa[1][k]), xr, 0.f);
      ua[2 * k] = dot2(pair_lo(wa[2][k], wa[3][k]), xr, 0.f); ua[2 * k + 1] = dot2(pair_hi(wa[2][k], wa[3][k]), xr, 0.f);
    }
    float g4[4], u4[4];
#pragma unroll
    for (int k = 0; k < 4; ++k) { g4[k] = tr32(ga[k], ga[k + 4]); u4[k] = tr32(ua[k], ua[k + 4]); }
    const float g0 = tr16(g4[0], g4[2]), g1 = tr16(g4[1], g4[3]), v0 = tr16(u4[0], u4[2]), v1 = tr16(u4[1], u4[3]);
    *(LAS f32x2*)(ldf + L_P1 + sw * 128 + 2 * (8 * c8 + r8)) = f32x2{trd<XOR8>(g0, g1, (lane & 8) != 0), trd<XOR8>(v0, v1, (lane & 8) != 0)};
    lds_post(flag + 4, sw, lane, tagA, tagB);
    SCHED_FENCE();
  }
  k_rows<IG + WIN + 1, IQ + 1>(kb, rk, voff);
  {
    gq[0] = ld_gran(rs, lane * 16, WS_HQ + (h * 8 + 2 * sw) * 1024); gq[1] = ld_gran(rs, lane * 16, WS_HQ + (h * 8 + 2 * sw + 1) * 1024);
    SCHED_FENCE();
  }
  k_rows<IQ + 1, IQ + WIN + 1>(kb, rk, voff);
  {
    gather_finish<2, 1024>(gq, rs, lane * 16, WS_HQ + (h * 8 + 2 * sw) * 1024, tagA, tagB);
    *(LAS f32x2*)(ldf + L_P2 + sw * 128 + 2 * lane) = f32x2{u2f(gq[0][0]) + u2f(gq[1][0]), u2f(gq[0][2]) + u2f(gq[1][2])};
    lds_post(flag + 8, sw, lane, tagA, tagB);
    SCHED_FENCE();
  }
  k_rows<IQ + WIN + 1, NBACK>(kb, rk, voff);
  lds_wait4(flag + 8, lane, tagA, tagB);
  u32x4 q;
  {
    typedef float f32x4 __attribute__((ext_vector_type(4)));
    f32x4 a = *(const LAS f32x4*)(ldf + L_P2 + 8 * c), d = *(const LAS f32x4*)(ldf + L_P2 + 8 * c + 4);
#pragma unroll
    for (int ww = 1; ww < 4; ++ww) { a += *(const LAS f32x4*)(ldf + L_P2 + ww * 128 + 8 * c); d += *(const LAS f32x4*)(ldf + L_P2 + ww * 128 + 8 * c + 4); }
    q = u32x4{pack_bf16(a[0], a[1]), pack_bf16(a[2], a[3]), pack_bf16(d[0], d[1]), pack_bf16(d[2], d[3])};
  }
  float sc[4], xs[16];
#pragma unroll
  for (int st = 0; st < 64 + WIN - NBACK; ++st) {
    const int n = NBACK + st;
    const int lim = n + 1 - WIN < CR * (st + 1) ? n + 1 - WIN : CR * (st + 1);
    const int from = st == 0 ? 0 : (n - WIN < CR * st ? n - WIN : CR * st);
    if (from < n - WIN) PACE(WIN - 1);
    if (n < 64) kb[n] = ld_kv(rk, voff, n * 4096); else ring[(n - 64) % RING] = ld_kv(rv, voff, (n - 64) * 4096);
    SCHED_FENCE();
#pragma unroll
    for (int j = from; j < lim; ++j) {
      xs[j & 15] = qk_dot(kb[j], q);
      asm volatile("" : "+v"(xs[j & 15]));
      if ((j & 15) == 15) { sc[j >> 4] = score16(xs, c); asm volatile("" : "+v"(sc[j >> 4])); }
    }
    SCHED_FENCE();
  }
  static_assert(CR * (64 + WIN - NBACK) >= 64, "catch-up must finish inside the K phase");
  u32x4 wov[4], wdv[2], xg, gwv;
  const int gvoff = (lane < 8 * nu ? lane : 0) * 16;
  const gp4 pwo = (gp4)(wo_ + (size_t)(h * DH + 32 * sw + 2 * r8) * D + 64 * s + 8 * c8);
  const gp4 pwd0 = (gp4)(wd_ + (size_t)(8 * (u0 + sw) + c8) * D + 64 * s + 8 * r8);
  const gp4 pwd1 = (gp4)(wd_ + (size_t)(8 * (u0 + (sw + 4 < nu ? sw + 4 : sw)) + c8) * D + 64 * s + 8 * r8);
  const float mw = xrow_max(row_max(fmaxf(fmaxf(sc[0], sc[1]), fmaxf(sc[2], sc[3]))));
  float p0 = __builtin_amdgcn_exp2f(sc[0] - mw), p1 = __builtin_amdgcn_exp2f(sc[1] - mw);
  float p2 = __builtin_amdgcn_exp2f(sc[2] - mw), p3 = __builtin_amdgcn_exp2f(sc[3] - mw);
  {
    const float lw = xrow_sum(row_sum((p0 + p1) + (p2 + p3)));
    if (lane == 0) *(LAS f32x2*)(ldf + L_ML + 2 * sw) = f32x2{mw, lw};
    lds_post(flag + 12, sw, lane, tagA, tagB);
  }
  float acc[8];
#pragma unroll
  for (int k = 0; k < 8; ++k) acc[k] = 0.f;
#define SIDE(I, E, STMT) PVS<I, E>::run(ring, rv, voff, WIN * 4096, p0, acc); PACE(WIN - 1); STMT; SCHED_FENCE();
  SIDE(0, 1, gq[0] = ld_gran(rs, gvoff, WS_HG + (h * 8 + 2 * sw) * 1024))
  SIDE(1, 2, gq[1] = ld_gran(rs, gvoff, WS_HG + (h * 8 + 2 * sw + 1) * 1024))
  SIDE(2, 3, if (sw == 0) { xg = ((gp4)x_)[lane]; gwv = ((gp4)gw_)[lane]; })
  SIDE(3, 4, wdv[0] = ld_nt(pwd0))
  SIDE(4, 5, wdv[1] = ld_nt(pwd1))
  SIDE(5, 6, wov[0] = ld_nt(pwo))
  SIDE(6, 7, wov[1] = ld_nt(pwo + D / 8))
  SIDE(7, 8, wov[2] = ld_nt(pwo + 16 * D / 8))
  SIDE(8, 9, wov[3] = ld_nt(pwo + 17 * D / 8))
  PVS<9, 16>::run(ring, rv, voff, WIN * 4096, p0, acc);
  float gate = 0.f, omg = 0.f;
  {
    gather_finish<2, 1024>(gq, rs, gvoff, WS_HG + (h * 8 + 2 * sw) * 1024, tagA, tagB);
    *(LAS f32x2*)(ldf + L_P3 + sw * 128 + 2 * lane) = f32x2{u2f(gq[0][0]) + u2f(gq[1][0]), u2f(gq[0][2]) + u2f(gq[1][2])};
    lds_post(flag + 16, sw, lane, tagA, tagB);
    if (sw == 0) {
      float z = 0.f;
  #pragma unroll
      for (int k = 0; k < 4; ++k) {
        z += rbf(bflo(xg[k]) * bflo(gwv[k]));
        z += rbf(bfhi(xg[k]) * bfhi(gwv[k]));
      }
      z = rbf(xrow_sum(row_sum(z)));
      gate = rbf(1.0f / (1.0f + __expf(-z)));
      omg = rbf(1.0f - gate);
    }
    asm volatile("" : "+v"(gate), "+v"(omg));
  }
  SCHED_FENCE();
#pragma unroll 1
  for (int bt = 1; bt < 3; ++bt) {
    PVS<0, 16>::run(ring, rv, voff, (16 * bt + WIN) * 4096, p1, acc);
    p1 = p2;
  }
  unsigned wp[16];
#pragma unroll
  for (int k = 0; k < 4; ++k) {
    wp[2 * k] = pair_lo(wov[0][k], wov[1][k]); wp[2 * k + 1] = pair_hi(wov[0][k], wov[1][k]);
    wp[8 + 2 * k] = pair_lo(wov[2][k], wov[3][k]); wp[8 + 2 * k + 1] = pair_hi(wov[2][k], wov[3][k]);
  }
#pragma unroll
  for (int k = 0; k < 16; ++k) asm volatile("" : "+v"(wp[k]));
  u32x4 gm[1];
  gm[0] = ld_gran(rs, c8 * 128, WS_ML + h * 1024);
  {
    lds_wait4(flag + 16, lane, tagA, tagB);
    f32x2 t0 = *(const LAS f32x2*)(ldf + L_P3 + 2 * (8 * sw + c8)), t1 = *(const LAS f32x2*)(ldf + L_P3 + 2 * (8 * sw + 32 + c8));
#pragma unroll
    for (int ww = 1; ww < 4; ++ww) {
      t0 += *(const LAS f32x2*)(ldf + L_P3 + ww * 128 + 2 * (8 * sw + c8)); t1 += *(const LAS f32x2*)(ldf + L_P3 + ww * 128 + 2 * (8 * sw + 32 + c8));
    }
    const float h0 = rbf(rbf(t0.x) * rbf(t0.y)), h1 = sw + 4 < nu ? rbf(rbf(t1.x) * rbf(t1.y)) : 0.f;
    float y[8];
#pragma unroll
    for (int k = 0; k < 4; ++k) {
      y[2 * k] = fmaf(h0, bflo(wdv[0][k]), h1 * bflo(wdv[1][k]));
      y[2 * k + 1] = fmaf(h0, bfhi(wdv[0][k]), h1 * bfhi(wdv[1][k]));
    }
    ldf[L_Y + 256 + sw * 64 + lane] = tr8(y, c8);
  }
  SCHED_FENCE();
  PVS<0, 10>::run(ring, rv, voff, (48 + WIN) * 4096, p3, acc);
  float f;
  int half;
  {
    gather_finish<1, 0>(gm, rs, c8 * 128, WS_ML + h * 1024, tagA, tagB);
    const float ms = u2f(gm[0][0]);
    float Mh = fmaxf(ms, dpp<XOR1>(ms)); Mh = fmaxf(Mh, dpp<XOR2>(Mh)); Mh = fmaxf(Mh, dpp<XOR7>(Mh));
    half = __builtin_popcountll(__builtin_amdgcn_ballot_w64((int)gm[0][2] < 0) & 0xffull) >= VOTES ? 0 : 1;
    if ((sw | lane) == 0) ldi[L_HALF] = half;
    half *= 8192;
    float Lh = __builtin_amdgcn_exp2f(ms - Mh) * fabsf(u2f(gm[0][2])); Lh += dpp<XOR1>(Lh); Lh += dpp<XOR2>(Lh); Lh += dpp<XOR7>(Lh);
    f = __builtin_amdgcn_exp2f(mw - Mh) / Lh;
    asm volatile("" : "+v"(f));
  }
  SCHED_FENCE();
  PVS<10, 16>::run(ring, rv, voff, (48 + WIN) * 4096, p3, acc);
  {
    const float t0 = tr32(acc[0], acc[4]), t1 = tr32(acc[1], acc[5]), t2 = tr32(acc[2], acc[6]), t3 = tr32(acc[3], acc[7]);
    *(LAS f32x2*)(ldf + L_OP + sw * 128 + 8 * c + 2 * g) = f32x2{tr16(t0, t2) * f, tr16(t1, t3) * f};
  }
  __syncthreads();
  {
    u32x4 gr[2];
    gather<2, 128, 0>(gr, rs, c8 * 1024 + r8 * 16, WS_H1 + h * 16384 + half + sw * 256, tagA, tagB);
    float o[4] = {u2f(gr[0][0]), u2f(gr[0][2]), u2f(gr[1][0]), u2f(gr[1][2])};
#pragma unroll
    for (int k = 0; k < 4; ++k) o[k] += dpp<XOR1>(o[k]);
#pragma unroll
    for (int k = 0; k < 4; ++k) o[k] += dpp<XOR2>(o[k]);
#pragma unroll
    for (int k = 0; k < 4; ++k) o[k] += dpp<XOR7>(o[k]);
    const unsigned oa = pack_bf16(o[0], o[1]), ob = pack_bf16(o[2], o[3]);
    float y[8];
#pragma unroll
    for (int k = 0; k < 8; ++k) y[k] = dot2(wp[8 + k], ob, dot2(wp[k], oa, 0.f));
    const float t0 = tr32(y[0], y[4]), t1 = tr32(y[1], y[5]), t2 = tr32(y[2], y[6]), t3 = tr32(y[3], y[7]);
    ldf[L_Y + sw * 64 + 8 * c8 + r8] = trd<XOR8>(tr16(t0, t2), tr16(t1, t3), (lane & 8) != 0);
  }
  __syncthreads();
  if (sw == 0) {
    u32x4 gr[1];
    gather<1, 0, 0>(gr, rs, (lane >> 4) * 16384 + ((lane >> 1) & 7) * 1024 + (lane & 1) * 16, WS_H2 + s * 65536 + half + h * 32, tagA, tagB);
    float as = u2f(gr[0][0]), bs = u2f(gr[0][2]);
    as += dpp<XOR2>(as); bs += dpp<XOR2>(bs);
    as += dpp<ROR4>(as); bs += dpp<ROR4>(bs);
    as += dpp<XOR8>(as); bs += dpp<XOR8>(bs);
    as = rbf(xrow_sum(as)); bs = rbf(xrow_sum(bs));
    const float res = rbf(gate * as) + rbf(omg * bs);
    const unsigned o2 = pack_bf16(res, dpp<XOR1>(res));
    if (lane == 0) {
      *(GAS unsigned*)(out_ + 64 * s + 2 * h) = o2;
      if (b == 0) __builtin_amdgcn_raw_buffer_store_b64(u32x2{epA, epB}, rs, 0, WS_EPOCH, AUX_SC1);
    }
  }
}

extern "C" void kernel_launch(void* const* d_in, const int* in_sizes, int n_in, void* d_out, int out_size, void* d_ws,
                              size_t ws_size, hipStream_t stream) {
  (void)in_sizes; (void)n_in; (void)out_size; (void)ws_size;
  typedef const unsigned short* cp;
  Params p;
  p.x = (cp)d_in[0]; p.k = (cp)d_in[1]; p.v = (cp)d_in[2]; p.wq = (cp)d_in[3]; p.wo = (cp)d_in[4];
  p.wg = (cp)d_in[6]; p.wu = (cp)d_in[7]; p.wd = (cp)d_in[8]; p.gw = (cp)d_in[9];
  p.out = (unsigned short*)d_out; p.ws = (unsigned char*)d_ws;
  fused_kernel<<<NWG, NTHR, 0, stream>>>(p);
}
```

```cpp
#include <hip/hip_runtime.h>
#include <hip/hip_bf16.h>
#include <stdint.h>

typedef unsigned u32x4 __attribute__((ext_vector_type(4)));
typedef unsigned u32x2 __attribute__((ext_vector_type(2)));
typedef float f32x2 __attribute__((ext_vector_type(2)));
typedef __bf16 bf16x2 __attribute__((ext_vector_type(2)));
#define GAS __attribute__((address_space(1)))
#define LAS __attribute__((address_space(3)))
typedef const GAS u32x4* gp4;

constexpr int D = 512, NH = 32, SK = 8192, DH = 128, FF = 1376;
constexpr int NTHR = 320, NWG = 256;
constexpr int KEYS_WG = 1024;
constexpr int WS_PR = 0;
constexpr int WS_H1 = WS_PR + 4 * 16384;
constexpr int WS_H2 = WS_H1 + NH * 16384;
constexpr int WS_HQ = WS_H2 + 32 * 16384;
constexpr int WS_HG = WS_HQ + NWG * 1024;
constexpr int WS_ML = WS_HG + NWG * 1024;
constexpr int WS_EPOCH = WS_ML + NWG * 128;
constexpr int WS_BYTES = WS_EPOCH + 128;
static_assert(WS_BYTES <= (2 << 20), "one 2 MiB page");
constexpr int WS_OOB = 0x04000000;
constexpr int AUX_SC1 = 16, AUX_NT = 2;
constexpr unsigned SPIN_CAP = 1u << 18;
constexpr unsigned TAG_A = 0x5bd1e995u, TAG_B = 0xc2b2ae35u;
constexpr int L_FLAG = 0;
constexpr int L_ML = 40;
constexpr int L_HALF = 48;
constexpr int L_P0 = 64;
constexpr int L_P1 = L_P0 + 512;
constexpr int L_P2 = L_P1 + 512;
constexpr int L_P3 = L_P2 + 512;
constexpr int L_OP = L_P3 + 512;
constexpr int L_Y = L_OP + 512;
constexpr int L_END = L_Y + 512;

constexpr int WIN = 7;
constexpr int NS = 12;
constexpr int NBACK = 56;
constexpr int CR = 8;
constexpr int IG = 30;
constexpr int IQ = 38;
constexpr int ES = 3;
constexpr int GP = 9 + WIN, YP = 44, FP = 58;
constexpr int NEAR_THR = 110;
constexpr int VOTES = 3;

__device__ __forceinline__ float dot2(unsigned a, unsigned b, float c) {
  return __builtin_amdgcn_fdot2_f32_bf16(__builtin_bit_cast(bf16x2, a), __builtin_bit_cast(bf16x2, b), c, false);
}
__device__ __forceinline__ float bflo(unsigned u) { return __builtin_bit_cast(float, u << 16); }
__device__ __forceinline__ float bfhi(unsigned u) { return __builtin_bit_cast(float, u & 0xffff0000u); }
__device__ __forceinline__ unsigned pack_bf16(float lo, float hi) {
  bf16x2 r; r.x = (__bf16)lo; r.y = (__bf16)hi; return __builtin_bit_cast(unsigned, r);
}
__device__ __forceinline__ float rbf(float x) { return (float)(__bf16)x; }
__device__ __forceinline__ unsigned pair_lo(unsigned r0, unsigned r1) { return __builtin_amdgcn_perm(r1, r0, 0x05040100u); }
__device__ __forceinline__ unsigned pair_hi(unsigned r0, unsigned r1) { return __builtin_amdgcn_perm(r1, r0, 0x07060302u); }
template <int CTRL> __device__ __forceinline__ float dpp(float x) {
  return __builtin_bit_cast(float, __builtin_amdgcn_mov_dpp(__builtin_bit_cast(int, x), CTRL, 0xf, 0xf, true));
}
constexpr int XOR1 = 0xB1, XOR2 = 0x4E, XOR7 = 0x141, XOR8 = 0x128, ROR4 = 0x124;
__device__ __forceinline__ unsigned f2u(float x) { return __builtin_bit_cast(unsigned, x); }
__device__ __forceinline__ float u2f(unsigned x) { return __builtin_bit_cast(float, x); }
__device__ __forceinline__ float tr32(float a, float b) {
  auto r = __builtin_amdgcn_permlane32_swap(f2u(a), f2u(b), false, false);
  return u2f(r[0]) + u2f(r[1]);
}
__device__ __forceinline__ float tr16(float a, float b) {
  auto r = __builtin_amdgcn_permlane16_swap(f2u(a), f2u(b), false, false);
  return u2f(r[0]) + u2f(r[1]);
}
__device__ __forceinline__ float xrow_sum(float x) { x = tr16(x, x); return tr32(x, x); }
__device__ __forceinline__ float xrow_max(float x) {
  auto s = __builtin_amdgcn_permlane16_swap(f2u(x), f2u(x), false, false);
  x = fmaxf(u2f(s[0]), u2f(s[1]));
  auto t = __builtin_amdgcn_permlane32_swap(f2u(x), f2u(x), false, false);
  return fmaxf(u2f(t[0]), u2f(t[1]));
}
__device__ __forceinline__ float row_sum(float x) {
  x += dpp<XOR1>(x); x += dpp<XOR2>(x); x += dpp<XOR7>(x); x += dpp<XOR8>(x); return x;
}
__device__ __forceinline__ float row_max(float x) {
  x = fmaxf(x, dpp<XOR1>(x)); x = fmaxf(x, dpp<XOR2>(x)); x = fmaxf(x, dpp<XOR7>(x)); x = fmaxf(x, dpp<XOR8>(x)); return x;
}
template <int CTRL> __device__ __forceinline__ float trd(float lo, float hi, bool sel) {
  const float t1 = lo + dpp<CTRL>(lo), t2 = hi + dpp<CTRL>(hi);
  return sel ? t2 : t1;
}
__device__ __forceinline__ float tr8(const float (&y)[8], int c8) {
  float a[4], b[2];
#pragma unroll
  for (int i = 0; i < 4; ++i) a[i] = trd<XOR7>(y[i], y[i + 4], (c8 & 4) != 0);
#pragma unroll
  for (int i = 0; i < 2; ++i) b[i] = trd<XOR2>(a[i], a[i + 2], (c8 & 2) != 0);
  return trd<XOR1>(b[0], b[1], (c8 & 1) != 0);
}
__device__ __forceinline__ u32x4 ld_nt(gp4 p) { return __builtin_nontemporal_load(p); }
__device__ __forceinline__ u32x4 ld_kv(__amdgpu_buffer_rsrc_t r, int voff, int soff) {
  return __builtin_amdgcn_raw_buffer_load_b128(r, voff, soff, AUX_NT);
}
#define PACE(n) asm volatile("s_waitcnt vmcnt(%0)" ::"i"(n) : "memory")
#define LDS_DRAIN() asm volatile("s_waitcnt lgkmcnt(0)" ::: "memory")
#define SCHED_FENCE() __builtin_amdgcn_sched_barrier(0)
__device__ __forceinline__ u32x4 ld_gran(__amdgpu_buffer_rsrc_t r, int voff, int soff) {
  asm volatile("" : "+v"(voff)::"memory");
  return __builtin_amdgcn_raw_buffer_load_b128(r, voff, soff, AUX_SC1);
}
__device__ __forceinline__ void st_gran(__amdgpu_buffer_rsrc_t r, int voff, int soff, float a, float b, unsigned tagA, unsigned tagB) {
  __builtin_amdgcn_raw_buffer_store_b128(u32x4{f2u(a), tagA, f2u(b), tagB}, r, voff, soff, AUX_SC1);
}
template <int N, int STRIDE, int SLEEP>
__device__ __forceinline__ void gather(u32x4 (&gr)[N], __amdgpu_buffer_rsrc_t r, int voff, int base, unsigned tagA, unsigned tagB) {
  static_assert(N == 1 || N == 2, "");
  unsigned long long m[N];
#pragma unroll
  for (int i = 0; i < N; ++i) { m[i] = ~0ull; gr[i] = u32x4{0u, 0u, 0u, 0u}; }
#pragma unroll 1
  for (unsigned spins = 0;; ++spins) {
    if constexpr (N == 1)
      asm volatile("s_mov_b64 exec, %1\n\tbuffer_load_dwordx4 %0, %2, %3, %4 offen sc1\n\ts_mov_b64 exec, -1\n\ts_waitcnt vmcnt(0)"
                   : "+v"(gr[0]) : "s"(m[0]), "v"(voff), "s"(r), "s"(base) : "memory");
    else
      asm volatile("s_mov_b64 exec, %2\n\ts_cbranch_execz 1f\n\tbuffer_load_dwordx4 %0, %4, %5, %6 offen sc1\n1:\n\t"
                   "s_mov_b64 exec, %3\n\ts_cbranch_execz 2f\n\tbuffer_load_dwordx4 %1, %4, %5, %7 offen sc1\n2:\n\t"
                   "s_mov_b64 exec, -1\n\ts_waitcnt vmcnt(0)"
                   : "+v"(gr[0]), "+v"(gr[N - 1]) : "s"(m[0]), "s"(m[N - 1]), "v"(voff), "s"(r), "s"(base), "s"(base + STRIDE) : "memory");
    unsigned long long missing = 0ull;
#pragma unroll
    for (int i = 0; i < N; ++i) {
      m[i] = __builtin_amdgcn_ballot_w64((gr[i][1] != tagA) | (gr[i][3] != tagB));
      missing |= m[i];
    }
    if (missing == 0ull || spins >= SPIN_CAP) break;
    if (SLEEP) __builtin_amdgcn_s_sleep(SLEEP);
  }
}
template <int N, int STRIDE>
__device__ __forceinline__ void gather_finish(u32x4 (&gr)[N], __amdgpu_buffer_rsrc_t r, int voff, int base, unsigned tagA, unsigned tagB) {
  bool bad = false;
#pragma unroll
  for (int i = 0; i < N; ++i) bad = bad | (gr[i][1] != tagA) | (gr[i][3] != tagB);
  if (__builtin_amdgcn_ballot_w64(bad) != 0ull) gather<N, STRIDE, 2>(gr, r, voff, base, tagA, tagB);
}
typedef volatile LAS unsigned long long* lflag;
__device__ __forceinline__ bool lds_all4(lflag grp, int lane, unsigned tagA, unsigned tagB) {
  const unsigned long long f = grp[lane & 3];
  const bool ok = __builtin_amdgcn_ballot_w64(((unsigned)f != tagA) | ((unsigned)(f >> 32) != tagB)) == 0ull;
  asm volatile("" ::: "memory");
  return ok;
}
template <int SLEEP = 1>
__device__ __forceinline__ void lds_wait4(lflag grp, int lane, unsigned tagA, unsigned tagB) {
#pragma unroll 1
  for (unsigned spins = 0; !lds_all4(grp, lane, tagA, tagB) && spins < (SPIN_CAP << 4); ++spins) __builtin_amdgcn_s_sleep(SLEEP);
}
__device__ __forceinline__ void lds_post(lflag grp, int sw, int lane, unsigned tagA, unsigned tagB) {
  LDS_DRAIN();
  if (lane == 0) grp[sw] = ((unsigned long long)tagB << 32) | tagA;
}

__device__ __forceinline__ float qk_dot(const u32x4& kk, const u32x4& q) {
  float d = dot2(kk[0], q[0], 0.f);
  d = dot2(kk[1], q[1], d); d = dot2(kk[2], q[2], d); return dot2(kk[3], q[3], d);
}
__device__ __forceinline__ float score16(const float (&xs)[16], int c) {
  float a[8], b[4], d[2];
#pragma unroll
  for (int i = 0; i < 8; ++i) a[i] = trd<XOR8>(xs[i], xs[i + 8], (c & 8) != 0);
#pragma unroll
  for (int i = 0; i < 4; ++i) b[i] = trd<XOR7>(a[i], a[i + 4], (c & 4) != 0);
#pragma unroll
  for (int i = 0; i < 2; ++i) d[i] = trd<XOR2>(b[i], b[i + 2], (c & 2) != 0);
  const float s = trd<XOR1>(d[0], d[1], (c & 1) != 0);
  return rbf(rbf(s) * 0.088388347648318440f) * 1.4426950408889634f;
}
template <int I> __device__ __forceinline__ void pv_step(const u32x4& v, float p, float (&acc)[8]) {
  const float pb = dpp<0x150 + I>(p);
#pragma unroll
  for (int k = 0; k < 4; ++k) {
    acc[2 * k] = fmaf(pb, bflo(v[k]), acc[2 * k]);
    acc[2 * k + 1] = fmaf(pb, bfhi(v[k]), acc[2 * k + 1]);
  }
  asm volatile("" : "+v"(acc[0]), "+v"(acc[1]), "+v"(acc[2]), "+v"(acc[3]), "+v"(acc[4]), "+v"(acc[5]), "+v"(acc[6]), "+v"(acc[7]));
}
constexpr int v_issued(int i) {
  const int n = i + 1 + WIN > WIN + ES ? i + 1 + WIN : WIN + ES;
  return n < 64 ? n : 64;
}
template <int I, int E> struct PVS {
  __device__ static __forceinline__ void run(u32x4 (&vb)[64], __amdgpu_buffer_rsrc_t rv, int voff, const float (&p)[4], float (&acc)[8]) {
    constexpr int from = I == 0 ? WIN + ES : v_issued(I - 1);
#pragma unroll
    for (int n = from; n < v_issued(I); ++n) { vb[n] = ld_kv(rv, voff, n * 4096); SCHED_FENCE(); }
    pv_step<I & 15>(vb[I], p[I >> 4], acc);
    SCHED_FENCE();
    PVS<I + 1, E>::run(vb, rv, voff, p, acc);
  }
};
template <int E> struct PVS<E, E> {
  __device__ static __forceinline__ void run(u32x4 (&)[64], __amdgpu_buffer_rsrc_t, int, const float (&)[4], float (&)[8]) {}
};

template <int FROM, int TO>
__device__ __forceinline__ void k_rows(u32x4 (&kb)[64], __amdgpu_buffer_rsrc_t rk, int voff) {
#pragma unroll
  for (int i = FROM; i < TO; ++i) { PACE(WIN - 1); kb[i] = ld_kv(rk, voff, i * 4096); SCHED_FENCE(); }
}

struct Params {
  const unsigned short *x, *k, *v, *wq, *wo, *wg, *wu, *wd, *gw;
  unsigned short* out;
  unsigned char* ws;
};
static_assert(sizeof(Params) == 88, "no padding");

__global__ void __launch_bounds__(NTHR, 2) fused_kernel(Params prm) {
  const unsigned short* __restrict__ x_ = prm.x; const unsigned short* __restrict__ k_ = prm.k; const unsigned short* __restrict__ v_ = prm.v;
  const unsigned short* __restrict__ wq_ = prm.wq; const unsigned short* __restrict__ wo_ = prm.wo; const unsigned short* __restrict__ wg_ = prm.wg;
  const unsigned short* __restrict__ wu_ = prm.wu; const unsigned short* __restrict__ wd_ = prm.wd; const unsigned short* __restrict__ gw_ = prm.gw;
  GAS unsigned short* out_ = (GAS unsigned short*)prm.out; unsigned char* ws_ = prm.ws;
  __shared__ __attribute__((aligned(16))) unsigned lds_raw[L_END];
  LAS float* const ldf = (LAS float*)lds_raw;
  const lflag flag = (lflag)((LAS unsigned*)lds_raw + L_FLAG);

  asm volatile("" ::"s"(x_), "s"(k_), "s"(v_), "s"(wq_), "s"(wo_), "s"(wg_), "s"(wu_), "s"(wd_), "s"(gw_), "s"(out_), "s"(ws_));
  const int b = blockIdx.x;
  const int h = (b & 7) * 4 + ((b >> 3) & 3), s = b >> 5;
  const int tid = threadIdx.x, lane = tid & 63, sw = __builtin_amdgcn_readfirstlane(tid >> 6) - 1;
  const int g = lane >> 4, c = lane & 15, r8 = lane >> 3, c8 = lane & 7;
  const int nu = h < 12 ? 6 : 5, u0 = h < 12 ? 6 * h : 72 + 5 * (h - 12);
  unsigned char* const ws2 = (unsigned char*)(((unsigned long long)ws_ + ((2ull << 20) - 1)) & ~((2ull << 20) - 1));
  const __amdgpu_buffer_rsrc_t rs = __builtin_amdgcn_make_buffer_rsrc((void*)ws2, 0, WS_BYTES, 0x00020000);
  LAS int* const ldi = (LAS int*)lds_raw;
  int vote = 0;
  if (sw < 0) {
    unsigned long long ta, tb;
    unsigned p0, p1;
    asm volatile("buffer_load_dword %2, off, %4, %5 sc1\n\tbuffer_load_dword %3, off, %4, %6 sc1\n\t"
                 "s_waitcnt vmcnt(1)\n\ts_memtime %0\n\ts_waitcnt vmcnt(0) lgkmcnt(0)\n\ts_memtime %1\n\ts_waitcnt lgkmcnt(0)"
                 : "=&s"(ta), "=&s"(tb), "=&v"(p0), "=&v"(p1)
                 : "s"(rs), "s"(WS_PR + (b >> 6) * 16384 + (b & 63) * 128), "s"(WS_PR + 8192 + (b >> 6) * 16384 + (b & 63) * 128) : "memory");
    vote = (unsigned)tb - (unsigned)ta > (unsigned)NEAR_THR;
  }
  int role = sw;
  asm volatile("" : "+s"(role));
  if (role < 0) {
    const u32x2 ep = __builtin_amdgcn_raw_buffer_load_b64(rs, 0, WS_EPOCH, AUX_SC1);
    const unsigned tagA = (__builtin_amdgcn_readfirstlane(ep[0]) + 1u) ^ TAG_A, tagB = (__builtin_amdgcn_readfirstlane(ep[1]) + 1u) ^ TAG_B;
    auto sum4 = [&](int base) {
      f32x2 t = *(const LAS f32x2*)(ldf + base + 2 * lane);
#pragma unroll
      for (int ww = 1; ww < 4; ++ww) t += *(const LAS f32x2*)(ldf + base + ww * 128 + 2 * lane);
      return t;
    };
    lds_wait4<2>(flag, lane, tagA, tagB);
    { const f32x2 t = sum4(L_P0); st_gran(rs, lane * 16, WS_HQ + (h * 8 + s) * 1024, t.x, t.y, tagA, tagB); }
    lds_wait4<8>(flag + 4, lane, tagA, tagB);
    { const f32x2 t = sum4(L_P1); st_gran(rs, lane < 8 * nu ? lane * 16 : WS_OOB, WS_HG + (h * 8 + s) * 1024, t.x, t.y, tagA, tagB); }
    lds_wait4<8>(flag + 12, lane, tagA, tagB);
    {
      const f32x2 ml = *(const LAS f32x2*)(ldf + L_ML + 2 * (lane & 3));
      float M = fmaxf(ml.x, dpp<XOR1>(ml.x)); M = fmaxf(M, dpp<XOR2>(M));
      float L = __builtin_amdgcn_exp2f(ml.x - M) * ml.y; L += dpp<XOR1>(L); L += dpp<XOR2>(L);
      st_gran(rs, lane == 0 ? 0 : WS_OOB, WS_ML + (h * 8 + s) * 128, M, vote ? -L : L, tagA, tagB);
    }
    __syncthreads();
    const int half = __builtin_amdgcn_readfirstlane(ldi[L_HALF]) * 8192;
    { const f32x2 t = sum4(L_OP); st_gran(rs, lane * 16, WS_H1 + h * 16384 + half + s * 1024, t.x, t.y, tagA, tagB); }
    __syncthreads();
    const float a = (ldf[L_Y + lane] + ldf[L_Y + 64 + lane]) + (ldf[L_Y + 128 + lane] + ldf[L_Y + 192 + lane]);
    const float bb = (ldf[L_Y + 256 + lane] + ldf[L_Y + 320 + lane]) + (ldf[L_Y + 384 + lane] + ldf[L_Y + 448 + lane]);
    const int o2 = WS_H2 + (s * 4 + (h >> 3)) * 16384 + (h & 7) * 1024;
    st_gran(rs, lane * 16, o2 + (8192 - half), a, bb, tagA, tagB);
    st_gran(rs, lane * 16, o2 + half, a, bb, tagA, tagB);
    return;
  }

  const size_t kvoff = ((size_t)h * SK + s * KEYS_WG + sw * 4) * DH;
  constexpr int KVBYTES = KEYS_WG * DH * 2 - 3 * 1024;
  const __amdgpu_buffer_rsrc_t rk = __builtin_amdgcn_make_buffer_rsrc((void*)(k_ + kvoff), 0, KVBYTES, 0x00020000);
  const __amdgpu_buffer_rsrc_t rv = __builtin_amdgcn_make_buffer_rsrc((void*)(v_ + kvoff), 0, KVBYTES, 0x00020000);
  const int voff = lane * 16;
  const int row0 = 64 * s + 16 * sw;

  u32x4 kb[64], vb[64], wa[4], gq[2];
  {
    const __amdgpu_buffer_rsrc_t rq = __builtin_amdgcn_make_buffer_rsrc((void*)(wq_ + (size_t)row0 * (NH * DH) + h * DH), 0, 16 * NH * DH * 2, 0x00020000);
#pragma unroll
    for (int i = 0; i < 4; ++i) { wa[i] = ld_kv(rq, g * (4 * NH * DH * 2) + c * 16, i * (NH * DH * 2)); SCHED_FENCE(); }
  }
  const u32x2 xq = *(const GAS u32x2*)(x_ + row0 + 4 * g);
  const unsigned xr = *(const GAS unsigned*)(x_ + row0 + 2 * r8);
  const u32x2 ep = __builtin_amdgcn_raw_buffer_load_b64(rs, 0, WS_EPOCH, AUX_SC1);
  SCHED_FENCE();
#pragma unroll
  for (int i = 0; i < NS; ++i) { kb[i] = ld_kv(rk, voff, i * 4096); SCHED_FENCE(); }
  const unsigned epA = __builtin_amdgcn_readfirstlane(ep[0]) + 1u, epB = __builtin_amdgcn_readfirstlane(ep[1]) + 1u;
  const unsigned tagA = epA ^ TAG_A, tagB = epB ^ TAG_B;
  {
    float qa[8];
#pragma unroll
    for (int k = 0; k < 4; ++k) {
      qa[2 * k] = dot2(pair_lo(wa[2][k], wa[3][k]), xq[1], dot2(pair_lo(wa[0][k], wa[1][k]), xq[0], 0.f));
      qa[2 * k + 1] = dot2(pair_hi(wa[2][k], wa[3][k]), xq[1], dot2(pair_hi(wa[0][k], wa[1][k]), xq[0], 0.f));
    }
    const float t0 = tr32(qa[0], qa[4]), t1 = tr32(qa[1], qa[5]), t2 = tr32(qa[2], qa[6]), t3 = tr32(qa[3], qa[7]);
    *(LAS f32x2*)(ldf + L_P0 + sw * 128 + 8 * c + 2 * g) = f32x2{tr16(t0, t2), tr16(t1, t3)};
    lds_post(flag, sw, lane, tagA, tagB);
  }
  SCHED_FENCE();
  static_assert(IG + WIN <= IQ && IQ + WIN < NBACK, "order of the side steps");
  k_rows<NS, IG + 1>(kb, rk, voff);
  {
    const size_t o = (size_t)(row0 + 2 * r8) * FF + (u0 + (c8 < nu ? c8 : nu - 1)) * 8;
    wa[0] = ld_nt((gp4)(wg_ + o)); wa[1] = ld_nt((gp4)(wg_ + o + FF)); wa[2] = ld_nt((gp4)(wu_ + o)); wa[3] = ld_nt((gp4)(wu_ + o + FF));
    SCHED_FENCE();
  }
  k_rows<IG + 1, IG + WIN + 1>(kb, rk, voff);
  {
    float ga[8], ua[8];
#pragma unroll
    for (int k = 0; k < 4; ++k) {
      ga[2 * k] = dot2(pair_lo(wa[0][k], wa[1][k]), xr, 0.f); ga[2 * k + 1] = dot2(pair_hi(wa[0][k], wa[1][k]), xr, 0.f);
      ua[2 * k] = dot2(pair_lo(wa[2][k], wa[3][k]), xr, 0.f); ua[2 * k + 1] = dot2(pair_hi(wa[2][k], wa[3][k]), xr, 0.f);
    }
    float g4[4], u4[4];
#pragma unroll
    for (int k = 0; k < 4; ++k) { g4[k] = tr32(ga[k], ga[k + 4]); u4[k] = tr32(ua[k], ua[k + 4]); }
    const float g0 = tr16(g4[0], g4[2]), g1 = tr16(g4[1], g4[3]), v0 = tr16(u4[0], u4[2]), v1 = tr16(u4[1], u4[3]);
    *(LAS f32x2*)(ldf + L_P1 + sw * 128 + 2 * (8 * c8 + r8)) = f32x2{trd<XOR8>(g0, g1, (lane & 8) != 0), trd<XOR8>(v0, v1, (lane & 8) != 0)};
    lds_post(flag + 4, sw, lane, tagA, tagB);
    SCHED_FENCE();
  }
  k_rows<IG + WIN + 1, IQ + 1>(kb, rk, voff);
  {
    gq[0] = ld_gran(rs, lane * 16, WS_HQ + (h * 8 + 2 * sw) * 1024); gq[1] = ld_gran(rs, lane * 16, WS_HQ + (h * 8 + 2 * sw + 1) * 1024);
    SCHED_FENCE();
  }
  k_rows<IQ + 1, IQ + WIN + 1>(kb, rk, voff);
  {
    gather_finish<2, 1024>(gq, rs, lane * 16, WS_HQ + (h * 8 + 2 * sw) * 1024, tagA, tagB);
    *(LAS f32x2*)(ldf + L_P2 + sw * 128 + 2 * lane) = f32x2{u2f(gq[0][0]) + u2f(gq[1][0]), u2f(gq[0][2]) + u2f(gq[1][2])};
    lds_post(flag + 8, sw, lane, tagA, tagB);
    SCHED_FENCE();
  }
  k_rows<IQ + WIN + 1, NBACK>(kb, rk, voff);
  lds_wait4(flag + 8, lane, tagA, tagB);
  u32x4 q;
  {
    typedef float f32x4 __attribute__((ext_vector_type(4)));
    f32x4 a = *(const LAS f32x4*)(ldf + L_P2 + 8 * c), d = *(const LAS f32x4*)(ldf + L_P2 + 8 * c + 4);
#pragma unroll
    for (int ww = 1; ww < 4; ++ww) { a += *(const LAS f32x4*)(ldf + L_P2 + ww * 128 + 8 * c); d += *(const LAS f32x4*)(ldf + L_P2 + ww * 128 + 8 * c + 4); }
    q = u32x4{pack_bf16(a[0], a[1]), pack_bf16(a[2], a[3]), pack_bf16(d[0], d[1]), pack_bf16(d[2], d[3])};
  }
  float sc[4], xs[16];
#pragma unroll
  for (int st = 0; st < 64 + WIN - NBACK; ++st) {
    const int n = NBACK + st;
    const int lim = n + 1 - WIN < CR * (st + 1) ? n + 1 - WIN : CR * (st + 1);
    const int from = st == 0 ? 0 : (n - WIN < CR * st ? n - WIN : CR * st);
    if (from < n - WIN) PACE(WIN - 1);
    if (n < 64) kb[n] = ld_kv(rk, voff, n * 4096); else vb[n - 64] = ld_kv(rv, voff, (n - 64) * 4096);
    SCHED_FENCE();
#pragma unroll
    for (int j = from; j < lim; ++j) {
      xs[j & 15] = qk_dot(kb[j], q);
      asm volatile("" : "+v"(xs[j & 15]));
      if ((j & 15) == 15) { sc[j >> 4] = score16(xs, c); asm volatile("" : "+v"(sc[j >> 4])); }
    }
    SCHED_FENCE();
  }
  static_assert(CR * (64 + WIN - NBACK) >= 64, "catch-up must finish inside the K phase");
  u32x4 wov[4], wdv[2], xg, gwv;
  const int gvoff = (lane < 8 * nu ? lane : 0) * 16;
  const gp4 pwo = (gp4)(wo_ + (size_t)(h * DH + 32 * sw + 2 * r8) * D + 64 * s + 8 * c8);
  const gp4 pwd0 = (gp4)(wd_ + (size_t)(8 * (u0 + sw) + c8) * D + 64 * s + 8 * r8);
  const gp4 pwd1 = (gp4)(wd_ + (size_t)(8 * (u0 + (sw + 4 < nu ? sw + 4 : sw)) + c8) * D + 64 * s + 8 * r8);
#pragma unroll
  for (int n = WIN; n < WIN + ES; ++n) { vb[n] = ld_kv(rv, voff, n * 4096); SCHED_FENCE(); }
  const float mw = fmaxf(xrow_max(row_max(fmaxf(fmaxf(sc[0], sc[1]), fmaxf(sc[2], sc[3])))), -3.0e38f);
  float p[4];
#pragma unroll
  for (int i = 0; i < 4; ++i) p[i] = __builtin_amdgcn_exp2f(sc[i] - mw);
  {
    const float lw = xrow_sum(row_sum((p[0] + p[1]) + (p[2] + p[3])));
    if (lane == 0) *(LAS f32x2*)(ldf + L_ML + 2 * sw) = f32x2{mw, lw};
    lds_post(flag + 12, sw, lane, tagA, tagB);
  }
  float acc[8];
#pragma unroll
  for (int k = 0; k < 8; ++k) acc[k] = 0.f;
#define SIDE(I, E, STMT) PVS<I, E>::run(vb, rv, voff, p, acc); PACE(WIN - 1); STMT; SCHED_FENCE();
  SIDE(0, 1, gq[0] = ld_gran(rs, gvoff, WS_HG + (h * 8 + 2 * sw) * 1024))
  SIDE(1, 2, gq[1] = ld_gran(rs, gvoff, WS_HG + (h * 8 + 2 * sw + 1) * 1024))
  SIDE(2, 3, if (sw == 0) { xg = ((gp4)x_)[lane]; gwv = ((gp4)gw_)[lane]; })
  SIDE(3, 4, wdv[0] = ld_nt(pwd0))
  SIDE(4, 5, wdv[1] = ld_nt(pwd1))
  SIDE(5, 6, wov[0] = ld_nt(pwo))
  SIDE(6, 7, wov[1] = ld_nt(pwo + D / 8))
  SIDE(7, 8, wov[2] = ld_nt(pwo + 16 * D / 8))
  SIDE(8, 9, wov[3] = ld_nt(pwo + 17 * D / 8))
  PVS<9, GP>::run(vb, rv, voff, p, acc);
  float gate = 0.f, omg = 0.f;
  {
    gather_finish<2, 1024>(gq, rs, gvoff, WS_HG + (h * 8 + 2 * sw) * 1024, tagA, tagB);
    *(LAS f32x2*)(ldf + L_P3 + sw * 128 + 2 * lane) = f32x2{u2f(gq[0][0]) + u2f(gq[1][0]), u2f(gq[0][2]) + u2f(gq[1][2])};
    lds_post(flag + 16, sw, lane, tagA, tagB);
    if (sw == 0) {
      float z = 0.f;
#pragma unroll
      for (int k = 0; k < 4; ++k) {
        z += rbf(bflo(xg[k]) * bflo(gwv[k]));
        z += rbf(bfhi(xg[k]) * bfhi(gwv[k]));
      }
      z = rbf(xrow_sum(row_sum(z)));
      gate = rbf(1.0f / (1.0f + __expf(-z)));
      omg = rbf(1.0f - gate);
    }
    asm volatile("" : "+v"(gate), "+v"(omg));
  }
  SCHED_FENCE();
  PVS<GP, YP>::run(vb, rv, voff, p, acc);
  unsigned wp[16];
#pragma unroll
  for (int k = 0; k < 4; ++k) {
    wp[2 * k] = pair_lo(wov[0][k], wov[1][k]); wp[2 * k + 1] = pair_hi(wov[0][k], wov[1][k]);
    wp[8 + 2 * k] = pair_lo(wov[2][k], wov[3][k]); wp[8 + 2 * k + 1] = pair_hi(wov[2][k], wov[3][k]);
  }
#pragma unroll
  for (int k = 0; k < 16; ++k) asm volatile("" : "+v"(wp[k]));
  u32x4 gm[1];
  gm[0] = ld_gran(rs, c8 * 128, WS_ML + h * 1024);
  {
    lds_wait4(flag + 16, lane, tagA, tagB);
    f32x2 t0 = *(const LAS f32x2*)(ldf + L_P3 + 2 * (8 * sw + c8)), t1 = *(const LAS f32x2*)(ldf + L_P3 + 2 * (8 * sw + 32 + c8));
#pragma unroll
    for (int ww = 1; ww < 4; ++ww) {
      t0 += *(const LAS f32x2*)(ldf + L_P3 + ww * 128 + 2 * (8 * sw + c8)); t1 += *(const LAS f32x2*)(ldf + L_P3 + ww * 128 + 2 * (8 * sw + 32 + c8));
    }
    const float h0 = rbf(rbf(t0.x) * rbf(t0.y)), h1 = sw + 4 < nu ? rbf(rbf(t1.x) * rbf(t1.y)) : 0.f;
    float y[8];
#pragma unroll
    for (int k = 0; k < 4; ++k) {
      y[2 * k] = fmaf(h0, bflo(wdv[0][k]), h1 * bflo(wdv[1][k]));
      y[2 * k + 1] = fmaf(h0, bfhi(wdv[0][k]), h1 * bfhi(wdv[1][k]));
    }
    ldf[L_Y + 256 + sw * 64 + lane] = tr8(y, c8);
  }
  SCHED_FENCE();
  PVS<YP, FP>::run(vb, rv, voff, p, acc);
  float f;
  int half;
  {
    gather_finish<1, 0>(gm, rs, c8 * 128, WS_ML + h * 1024, tagA, tagB);
    const float ms = u2f(gm[0][0]);
    float Mh = fmaxf(ms, dpp<XOR1>(ms)); Mh = fmaxf(Mh, dpp<XOR2>(Mh)); Mh = fmaxf(Mh, dpp<XOR7>(Mh));
    half = __builtin_popcountll(__builtin_amdgcn_ballot_w64((int)gm[0][2] < 0) & 0xffull) >= VOTES ? 0 : 1;
    if ((sw | lane) == 0) ldi[L_HALF] = half;
    half *= 8192;
    float Lh = __builtin_amdgcn_exp2f(ms - Mh) * fabsf(u2f(gm[0][2])); Lh += dpp<XOR1>(Lh); Lh += dpp<XOR2>(Lh); Lh += dpp<XOR7>(Lh);
    f = __builtin_amdgcn_exp2f(mw - Mh) / Lh;
    asm volatile("" : "+v"(f));
  }
  SCHED_FENCE();
  PVS<FP, 64>::run(vb, rv, voff, p, acc);
  {
    const float t0 = tr32(acc[0], acc[4]), t1 = tr32(acc[1], acc[5]), t2 = tr32(acc[2], acc[6]), t3 = tr32(acc[3], acc[7]);
    *(LAS f32x2*)(ldf + L_OP + sw * 128 + 8 * c + 2 * g) = f32x2{tr16(t0, t2) * f, tr16(t1, t3) * f};
  }
  __syncthreads();
  {
    u32x4 gr[2];
    gather<2, 128, 0>(gr, rs, c8 * 1024 + r8 * 16, WS_H1 + h * 16384 + half + sw * 256, tagA, tagB);
    float o[4] = {u2f(gr[0][0]), u2f(gr[0][2]), u2f(gr[1][0]), u2f(gr[1][2])};
#pragma unroll
    for (int k = 0; k < 4; ++k) o[k] += dpp<XOR1>(o[k]);
#pragma unroll
    for (int k = 0; k < 4; ++k) o[k] += dpp<XOR2>(o[k]);
#pragma unroll
    for (int k = 0; k < 4; ++k) o[k] += dpp<XOR7>(o[k]);
    const unsigned oa = pack_bf16(o[0], o[1]), ob = pack_bf16(o[2], o[3]);
    float y[8];
#pragma unroll
    for (int k = 0; k < 8; ++k) y[k] = dot2(wp[8 + k], ob, dot2(wp[k], oa, 0.f));
    const float t0 = tr32(y[0], y[4]), t1 = tr32(y[1], y[5]), t2 = tr32(y[2], y[6]), t3 = tr32(y[3], y[7]);
    ldf[L_Y + sw * 64 + 8 * c8 + r8] = trd<XOR8>(tr16(t0, t2), tr16(t1, t3), (lane & 8) != 0);
  }
  __syncthreads();
  if (sw == 0) {
    u32x4 gr[1];
    gather<1, 0, 0>(gr, rs, (lane >> 4) * 16384 + ((lane >> 1) & 7) * 1024 + (lane & 1) * 16, WS_H2 + s * 65536 + half + h * 32, tagA, tagB);
    float as = u2f(gr[0][0]), bs = u2f(gr[0][2]);
    as += dpp<XOR2>(as); bs += dpp<XOR2>(bs);
    as += dpp<ROR4>(as); bs += dpp<ROR4>(bs);
    as += dpp<XOR8>(as); bs += dpp<XOR8>(bs);
    as = rbf(xrow_sum(as)); bs = rbf(xrow_sum(bs));
    const float res = rbf(gate * as) + rbf(omg * bs);
    const unsigned o2 = pack_bf16(res, dpp<XOR1>(res));
    if (lane == 0) {
      *(GAS unsigned*)(out_ + 64 * s + 2 * h) = o2;
      if (b == 0) __builtin_amdgcn_raw_buffer_store_b64(u32x2{epA, epB}, rs, 0, WS_EPOCH, AUX_SC1);
    }
  }
}

extern "C" void kernel_launch(void* const* d_in, const int* in_sizes, int n_in, void* d_out, int out_size, void* d_ws,
                              size_t ws_size, hipStream_t stream) {
  if (n_in != 10 || in_sizes[1] != NH * SK * DH || out_size != D || ws_size < (size_t)WS_OOB + (6u << 20)) return;
  typedef const unsigned short* cp;
  Params p;
  p.x = (cp)d_in[0]; p.k = (cp)d_in[1]; p.v = (cp)d_in[2]; p.wq = (cp)d_in[3]; p.wo = (cp)d_in[4];
  p.wg = (cp)d_in[6]; p.wu = (cp)d_in[7]; p.wd = (cp)d_in[8]; p.gw = (cp)d_in[9];
  p.out = (unsigned short*)d_out; p.ws = (unsigned char*)d_ws;
  fused_kernel<<<NWG, NTHR, 0, stream>>>(p);
}
```

```cpp
#include <hip/hip_runtime.h>
#include <hip/hip_bf16.h>
#include <stdint.h>

typedef unsigned u32x4 __attribute__((ext_vector_type(4)));
typedef unsigned u32x2 __attribute__((ext_vector_type(2)));
typedef float f32x2 __attribute__((ext_vector_type(2)));
typedef __bf16 bf16x2 __attribute__((ext_vector_type(2)));
#define GAS __attribute__((address_space(1)))
#define LAS __attribute__((address_space(3)))
typedef const GAS u32x4* gp4;

constexpr int D = 512, NH = 32, SK = 8192, DH = 128, FF = 1376;
constexpr int NTHR = 320, NWG = 256;
constexpr int KEYS_WG = 1024;
constexpr int WS_PR = 0;
constexpr int WS_H1 = WS_PR + 4 * 16384;
constexpr int WS_H2 = WS_H1 + NH * 16384;
constexpr int WS_HQ = WS_H2 + 32 * 16384;
constexpr int WS_HG = WS_HQ + NWG * 1024;
constexpr int WS_ML = WS_HG + NWG * 1024;
constexpr int WS_EPOCH = WS_ML + NWG * 128;
constexpr int WS_BYTES = WS_EPOCH + 128;
static_assert(WS_BYTES <= (2 << 20), "one 2 MiB page");
constexpr int WS_OOB = 0x04000000;
constexpr int AUX_SC1 = 16, AUX_NT = 2;
constexpr unsigned SPIN_CAP = 1u << 18;
constexpr unsigned TAG_A = 0x5bd1e995u, TAG_B = 0xc2b2ae35u;
constexpr int L_FLAG = 0;
constexpr int L_ML = 40;
constexpr int L_HALF = 48;
constexpr int L_P0 = 64;
constexpr int L_P1 = L_P0 + 512;
constexpr int L_P2 = L_P1 + 512;
constexpr int L_P3 = L_P2 + 512;
constexpr int L_OP = L_P3 + 512;
constexpr int L_Y = L_OP + 512;
constexpr int L_END = L_Y + 512;

constexpr int WIN = 7;
constexpr int NS = 14;
constexpr int NBACK = 56;
constexpr int CR = 8;
constexpr int IG = 30;
constexpr int IQ = 38;
constexpr int ES = 3;
constexpr int GP = 9 + WIN, YP = 44, FP = 58;
constexpr int NEAR_THR = 110;
constexpr int VOTES = 3;

__device__ __forceinline__ float dot2(unsigned a, unsigned b, float c) {
  return __builtin_amdgcn_fdot2_f32_bf16(__builtin_bit_cast(bf16x2, a), __builtin_bit_cast(bf16x2, b), c, false);
}
__device__ __forceinline__ float bflo(unsigned u) { return __builtin_bit_cast(float, u << 16); }
__device__ __forceinline__ float bfhi(unsigned u) { return __builtin_bit_cast(float, u & 0xffff0000u); }
__device__ __forceinline__ unsigned pack_bf16(float lo, float hi) {
  bf16x2 r; r.x = (__bf16)lo; r.y = (__bf16)hi; return __builtin_bit_cast(unsigned, r);
}
__device__ __forceinline__ float rbf(float x) { return (float)(__bf16)x; }
__device__ __forceinline__ unsigned pair_lo(unsigned r0, unsigned r1) { return __builtin_amdgcn_perm(r1, r0, 0x05040100u); }
__device__ __forceinline__ unsigned pair_hi(unsigned r0, unsigned r1) { return __builtin_amdgcn_perm(r1, r0, 0x07060302u); }
template <int CTRL> __device__ __forceinline__ float dpp(float x) {
  return __builtin_bit_cast(float, __builtin_amdgcn_mov_dpp(__builtin_bit_cast(int, x), CTRL, 0xf, 0xf, true));
}
constexpr int XOR1 = 0xB1, XOR2 = 0x4E, XOR7 = 0x141, XOR8 = 0x128, ROR4 = 0x124;
__device__ __forceinline__ unsigned f2u(float x) { return __builtin_bit_cast(unsigned, x); }
__device__ __forceinline__ float u2f(unsigned x) { return __builtin_bit_cast(float, x); }
__device__ __forceinline__ float tr32(float a, float b) {
  auto r = __builtin_amdgcn_permlane32_swap(f2u(a), f2u(b), false, false);
  return u2f(r[0]) + u2f(r[1]);
}
__device__ __forceinline__ float tr16(float a, float b) {
  auto r = __builtin_amdgcn_permlane16_swap(f2u(a), f2u(b), false, false);
  return u2f(r[0]) + u2f(r[1]);
}
__device__ __forceinline__ float xrow_sum(float x) { x = tr16(x, x); return tr32(x, x); }
__device__ __forceinline__ float xrow_max(float x) {
  auto s = __builtin_amdgcn_permlane16_swap(f2u(x), f2u(x), false, false);
  x = fmaxf(u2f(s[0]), u2f(s[1]));
  auto t = __builtin_amdgcn_permlane32_swap(f2u(x), f2u(x), false, false);
  return fmaxf(u2f(t[0]), u2f(t[1]));
}
__device__ __forceinline__ float row_sum(float x) {
  x += dpp<XOR1>(x); x += dpp<XOR2>(x); x += dpp<XOR7>(x); x += dpp<XOR8>(x); return x;
}
__device__ __forceinline__ float row_max(float x) {
  x = fmaxf(x, dpp<XOR1>(x)); x = fmaxf(x, dpp<XOR2>(x)); x = fmaxf(x, dpp<XOR7>(x)); x = fmaxf(x, dpp<XOR8>(x)); return x;
}
template <int CTRL> __device__ __forceinline__ float trd(float lo, float hi, bool sel) {
  const float t1 = lo + dpp<CTRL>(lo), t2 = hi + dpp<CTRL>(hi);
  return sel ? t2 : t1;
}
__device__ __forceinline__ float tr8(const float (&y)[8], int c8) {
  float a[4], b[2];
#pragma unroll
  for (int i = 0; i < 4; ++i) a[i] = trd<XOR7>(y[i], y[i + 4], (c8 & 4) != 0);
#pragma unroll
  for (int i = 0; i < 2; ++i) b[i] = trd<XOR2>(a[i], a[i + 2], (c8 & 2) != 0);
  return trd<XOR1>(b[0], b[1], (c8 & 1) != 0);
}
__device__ __forceinline__ u32x4 ld_nt(gp4 p) { return __builtin_nontemporal_load(p); }
__device__ __forceinline__ u32x4 ld_kv(__amdgpu_buffer_rsrc_t r, int voff, int soff) {
  return __builtin_amdgcn_raw_buffer_load_b128(r, voff, soff, AUX_NT);
}
#define PACE(n) asm volatile("s_waitcnt vmcnt(%0)" ::"i"(n) : "memory")
#define LDS_DRAIN() asm volatile("s_waitcnt lgkmcnt(0)" ::: "memory")
#define SCHED_FENCE() __builtin_amdgcn_sched_barrier(0)
__device__ __forceinline__ u32x4 ld_gran(__amdgpu_buffer_rsrc_t r, int voff, int soff) {
  asm volatile("" : "+v"(voff)::"memory");
  return __builtin_amdgcn_raw_buffer_load_b128(r, voff, soff, AUX_SC1);
}
__device__ __forceinline__ void st_gran(__amdgpu_buffer_rsrc_t r, int voff, int soff, float a, float b, unsigned tagA, unsigned tagB) {
  __builtin_amdgcn_raw_buffer_store_b128(u32x4{f2u(a), tagA, f2u(b), tagB}, r, voff, soff, AUX_SC1);
}
template <int N, int STRIDE, int SLEEP>
__device__ __forceinline__ void gather(u32x4 (&gr)[N], __amdgpu_buffer_rsrc_t r, int voff, int base, unsigned tagA, unsigned tagB) {
  static_assert(N == 1 || N == 2, "");
  unsigned long long m[N];
#pragma unroll
  for (int i = 0; i < N; ++i) { m[i] = ~0ull; gr[i] = u32x4{0u, 0u, 0u, 0u}; }
#pragma unroll 1
  for (unsigned spins = 0;; ++spins) {
    if constexpr (N == 1)
      asm volatile("s_mov_b64 exec, %1\n\tbuffer_load_dwordx4 %0, %2, %3, %4 offen sc1\n\ts_mov_b64 exec, -1\n\ts_waitcnt vmcnt(0)"
                   : "+v"(gr[0]) : "s"(m[0]), "v"(voff), "s"(r), "s"(base) : "memory");
    else
      asm volatile("s_mov_b64 exec, %2\n\ts_cbranch_execz 1f\n\tbuffer_load_dwordx4 %0, %4, %5, %6 offen sc1\n1:\n\t"
                   "s_mov_b64 exec, %3\n\ts_cbranch_execz 2f\n\tbuffer_load_dwordx4 %1, %4, %5, %7 offen sc1\n2:\n\t"
                   "s_mov_b64 exec, -1\n\ts_waitcnt vmcnt(0)"
                   : "+v"(gr[0]), "+v"(gr[N - 1]) : "s"(m[0]), "s"(m[N - 1]), "v"(voff), "s"(r), "s"(base), "s"(base + STRIDE) : "memory");
    unsigned long long missing = 0ull;
#pragma unroll
    for (int i = 0; i < N; ++i) {
      m[i] = __builtin_amdgcn_ballot_w64((gr[i][1] != tagA) | (gr[i][3] != tagB));
      missing |= m[i];
    }
    if (missing == 0ull || spins >= SPIN_CAP) break;
    if (SLEEP) __builtin_amdgcn_s_sleep(SLEEP);
  }
}
template <int N, int STRIDE>
__device__ __forceinline__ void gather_finish(u32x4 (&gr)[N], __amdgpu_buffer_rsrc_t r, int voff, int base, unsigned tagA, unsigned tagB) {
  bool bad = false;
#pragma unroll
  for (int i = 0; i < N; ++i) bad = bad | (gr[i][1] != tagA) | (gr[i][3] != tagB);
  if (__builtin_amdgcn_ballot_w64(bad) != 0ull) gather<N, STRIDE, 2>(gr, r, voff, base, tagA, tagB);
}
typedef volatile LAS unsigned* lflag;
template <int SLEEP = 1>
__device__ __forceinline__ void lds_wait4(lflag grp, int lane) {
#pragma unroll 1
  for (unsigned spins = 0; __builtin_amdgcn_ballot_w64(grp[lane & 3] == 0u) != 0ull && spins < (SPIN_CAP << 4); ++spins) __builtin_amdgcn_s_sleep(SLEEP);
  asm volatile("" ::: "memory");
}
__device__ __forceinline__ void lds_post(lflag grp, int sw, int lane) {
  LDS_DRAIN();
  if (lane == 0) grp[sw] = 1u;
}

__device__ __forceinline__ float qk_dot(const u32x4& kk, const u32x4& q) {
  float d = dot2(kk[0], q[0], 0.f);
  d = dot2(kk[1], q[1], d); d = dot2(kk[2], q[2], d); return dot2(kk[3], q[3], d);
}
__device__ __forceinline__ float score16(const float (&xs)[16], int c) {
  float a[8], b[4], d[2];
#pragma unroll
  for (int i = 0; i < 8; ++i) a[i] = trd<XOR8>(xs[i], xs[i + 8], (c & 8) != 0);
#pragma unroll
  for (int i = 0; i < 4; ++i) b[i] = trd<XOR7>(a[i], a[i + 4], (c & 4) != 0);
#pragma unroll
  for (int i = 0; i < 2; ++i) d[i] = trd<XOR2>(b[i], b[i + 2], (c & 2) != 0);
  const float s = trd<XOR1>(d[0], d[1], (c & 1) != 0);
  return rbf(rbf(s) * 0.088388347648318440f) * 1.4426950408889634f;
}
template <int I> __device__ __forceinline__ void pv_step(const u32x4& v, float p, float (&acc)[8]) {
  const float pb = dpp<0x150 + I>(p);
#pragma unroll
  for (int k = 0; k < 4; ++k) {
    acc[2 * k] = fmaf(pb, bflo(v[k]), acc[2 * k]);
    acc[2 * k + 1] = fmaf(pb, bfhi(v[k]), acc[2 * k + 1]);
  }
  asm volatile("" : "+v"(acc[0]), "+v"(acc[1]), "+v"(acc[2]), "+v"(acc[3]), "+v"(acc[4]), "+v"(acc[5]), "+v"(acc[6]), "+v"(acc[7]));
}
constexpr int v_issued(int i) {
  const int n = i + 1 + WIN > WIN + ES ? i + 1 + WIN : WIN + ES;
  return n < 64 ? n : 64;
}
template <int I, int E> struct PVS {
  __device__ static __forceinline__ void run(u32x4 (&vb)[64], __amdgpu_buffer_rsrc_t rv, int voff, const float (&p)[4], float (&acc)[8]) {
    constexpr int from = I == 0 ? WIN + ES : v_issued(I - 1);
#pragma unroll
    for (int n = from; n < v_issued(I); ++n) { vb[n] = ld_kv(rv, voff, n * 4096); SCHED_FENCE(); }
    pv_step<I & 15>(vb[I], p[I >> 4], acc);
    SCHED_FENCE();
    PVS<I + 1, E>::run(vb, rv, voff, p, acc);
  }
};
template <int E> struct PVS<E, E> {
  __device__ static __forceinline__ void run(u32x4 (&)[64], __amdgpu_buffer_rsrc_t, int, const float (&)[4], float (&)[8]) {}
};

template <int FROM, int TO>
__device__ __forceinline__ void k_rows(u32x4 (&kb)[64], __amdgpu_buffer_rsrc_t rk, int voff) {
#pragma unroll
  for (int i = FROM; i < TO; ++i) { PACE(WIN - 1); kb[i] = ld_kv(rk, voff, i * 4096); SCHED_FENCE(); }
}

struct Params {
  const unsigned short *x, *k, *v, *wq, *wo, *wg, *wu, *wd, *gw;
  unsigned short* out;
  unsigned char* ws;
};
static_assert(sizeof(Params) == 88, "no padding");

__global__ void __launch_bounds__(NTHR, 2) fused_kernel(Params prm) {
  const unsigned short* __restrict__ x_ = prm.x; const unsigned short* __restrict__ k_ = prm.k; const unsigned short* __restrict__ v_ = prm.v;
  const unsigned short* __restrict__ wq_ = prm.wq; const unsigned short* __restrict__ wo_ = prm.wo; const unsigned short* __restrict__ wg_ = prm.wg;
  const unsigned short* __restrict__ wu_ = prm.wu; const unsigned short* __restrict__ wd_ = prm.wd; const unsigned short* __restrict__ gw_ = prm.gw;
  GAS unsigned short* out_ = (GAS unsigned short*)prm.out; unsigned char* ws_ = prm.ws;
  __shared__ __attribute__((aligned(16))) unsigned lds_raw[L_END];
  LAS float* const ldf = (LAS float*)lds_raw;
  const lflag flag = (lflag)((LAS unsigned*)lds_raw + L_FLAG);

  asm volatile("" ::"s"(x_), "s"(k_), "s"(v_), "s"(wq_), "s"(wo_), "s"(wg_), "s"(wu_), "s"(wd_), "s"(gw_), "s"(out_), "s"(ws_));
  const int b = blockIdx.x;
  const int h = (b & 7) * 4 + ((b >> 3) & 3), s = b >> 5;
  const int tid = threadIdx.x, lane = tid & 63, sw = __builtin_amdgcn_readfirstlane(tid >> 6) - 1;
  const int g = lane >> 4, c = lane & 15, r8 = lane >> 3, c8 = lane & 7;
  const int nu = h < 12 ? 6 : 5, u0 = h < 12 ? 6 * h : 72 + 5 * (h - 12);
  unsigned char* const ws2 = (unsigned char*)(((unsigned long long)ws_ + ((2ull << 20) - 1)) & ~((2ull << 20) - 1));
  const __amdgpu_buffer_rsrc_t rs = __builtin_amdgcn_make_buffer_rsrc((void*)ws2, 0, WS_BYTES, 0x00020000);
  LAS int* const ldi = (LAS int*)lds_raw;
  int vote = 0;
  if (sw < 0) {
    if (lane < 20) flag[lane] = 0u;
    unsigned long long ta, tb;
    unsigned p0, p1;
    asm volatile("buffer_load_dword %2, off, %4, %5 sc1\n\tbuffer_load_dword %3, off, %4, %6 sc1\n\t"
                 "s_waitcnt vmcnt(1)\n\ts_memtime %0\n\ts_waitcnt vmcnt(0) lgkmcnt(0)\n\ts_memtime %1\n\ts_waitcnt lgkmcnt(0)"
                 : "=&s"(ta), "=&s"(tb), "=&v"(p0), "=&v"(p1)
                 : "s"(rs), "s"(WS_PR + (b >> 6) * 16384 + (b & 63) * 128), "s"(WS_PR + 8192 + (b >> 6) * 16384 + (b & 63) * 128) : "memory");
    vote = (unsigned)tb - (unsigned)ta > (unsigned)NEAR_THR;
    __syncthreads();
  }
  int role = sw;
  asm volatile("" : "+s"(role));
  if (role < 0) {
    const u32x2 ep = __builtin_amdgcn_raw_buffer_load_b64(rs, 0, WS_EPOCH, AUX_SC1);
    const unsigned tagA = (__builtin_amdgcn_readfirstlane(ep[0]) + 1u) ^ TAG_A, tagB = (__builtin_amdgcn_readfirstlane(ep[1]) + 1u) ^ TAG_B;
    auto sum4 = [&](int base) {
      f32x2 t = *(const LAS f32x2*)(ldf + base + 2 * lane);
#pragma unroll
      for (int ww = 1; ww < 4; ++ww) t += *(const LAS f32x2*)(ldf + base + ww * 128 + 2 * lane);
      return t;
    };
    lds_wait4<2>(flag, lane);
    { const f32x2 t = sum4(L_P0); st_gran(rs, lane * 16, WS_HQ + (h * 8 + s) * 1024, t.x, t.y, tagA, tagB); }
    lds_wait4<8>(flag + 4, lane);
    { const f32x2 t = sum4(L_P1); st_gran(rs, lane < 8 * nu ? lane * 16 : WS_OOB, WS_HG + (h * 8 + s) * 1024, t.x, t.y, tagA, tagB); }
    lds_wait4<8>(flag + 12, lane);
    {
      const f32x2 ml = *(const LAS f32x2*)(ldf + L_ML + 2 * (lane & 3));
      float M = fmaxf(ml.x, dpp<XOR1>(ml.x)); M = fmaxf(M, dpp<XOR2>(M));
      float L = __builtin_amdgcn_exp2f(ml.x - M) * ml.y; L += dpp<XOR1>(L); L += dpp<XOR2>(L);
      st_gran(rs, lane == 0 ? 0 : WS_OOB, WS_ML + (h * 8 + s) * 128, M, vote ? -L : L, tagA, tagB);
    }
    __syncthreads();
    const int half = __builtin_amdgcn_readfirstlane(ldi[L_HALF]) * 8192;
    { const f32x2 t = sum4(L_OP); st_gran(rs, lane * 16, WS_H1 + h * 16384 + half + s * 1024, t.x, t.y, tagA, tagB); }
    __syncthreads();
    const float a = (ldf[L_Y + lane] + ldf[L_Y + 64 + lane]) + (ldf[L_Y + 128 + lane] + ldf[L_Y + 192 + lane]);
    const float bb = (ldf[L_Y + 256 + lane] + ldf[L_Y + 320 + lane]) + (ldf[L_Y + 384 + lane] + ldf[L_Y + 448 + lane]);
    const int o2 = WS_H2 + (s * 4 + (h >> 3)) * 16384 + (h & 7) * 1024;
    st_gran(rs, lane * 16, o2 + (8192 - half), a, bb, tagA, tagB);
    st_gran(rs, lane * 16, o2 + half, a, bb, tagA, tagB);
    return;
  }

  const size_t kvoff = ((size_t)h * SK + s * KEYS_WG + sw * 4) * DH;
  constexpr int KVBYTES = KEYS_WG * DH * 2 - 3 * 1024;
  const __amdgpu_buffer_rsrc_t rk = __builtin_amdgcn_make_buffer_rsrc((void*)(k_ + kvoff), 0, KVBYTES, 0x00020000);
  const __amdgpu_buffer_rsrc_t rv = __builtin_amdgcn_make_buffer_rsrc((void*)(v_ + kvoff), 0, KVBYTES, 0x00020000);
  const int voff = lane * 16;
  const int row0 = 64 * s + 16 * sw;

  u32x4 kb[64], vb[64], wa[4], gq[2];
  {
    const __amdgpu_buffer_rsrc_t rq = __builtin_amdgcn_make_buffer_rsrc((void*)(wq_ + (size_t)row0 * (NH * DH) + h * DH), 0, 16 * NH * DH * 2, 0x00020000);
#pragma unroll
    for (int i = 0; i < 4; ++i) { wa[i] = ld_kv(rq, g * (4 * NH * DH * 2) + c * 16, i * (NH * DH * 2)); SCHED_FENCE(); }
  }
  const u32x2 xq = *(const GAS u32x2*)(x_ + row0 + 4 * g);
  const unsigned xr = *(const GAS unsigned*)(x_ + row0 + 2 * r8);
  const u32x2 ep = __builtin_amdgcn_raw_buffer_load_b64(rs, 0, WS_EPOCH, AUX_SC1);
  SCHED_FENCE();
#pragma unroll
  for (int i = 0; i < NS; ++i) { kb[i] = ld_kv(rk, voff, i * 4096); SCHED_FENCE(); }
  __syncthreads();
  SCHED_FENCE();
  const unsigned epA = __builtin_amdgcn_readfirstlane(ep[0]) + 1u, epB = __builtin_amdgcn_readfirstlane(ep[1]) + 1u;
  const unsigned tagA = epA ^ TAG_A, tagB = epB ^ TAG_B;
  {
    float qa[8];
#pragma unroll
    for (int k = 0; k < 4; ++k) {
      qa[2 * k] = dot2(pair_lo(wa[2][k], wa[3][k]), xq[1], dot2(pair_lo(wa[0][k], wa[1][k]), xq[0], 0.f));
      qa[2 * k + 1] = dot2(pair_hi(wa[2][k], wa[3][k]), xq[1], dot2(pair_hi(wa[0][k], wa[1][k]), xq[0], 0.f));
    }
    const float t0 = tr32(qa[0], qa[4]), t1 = tr32(qa[1], qa[5]), t2 = tr32(qa[2], qa[6]), t3 = tr32(qa[3], qa[7]);
    *(LAS f32x2*)(ldf + L_P0 + sw * 128 + 8 * c + 2 * g) = f32x2{tr16(t0, t2), tr16(t1, t3)};
    lds_post(flag, sw, lane);
  }
  SCHED_FENCE();
  static_assert(IG + WIN <= IQ && IQ + WIN < NBACK, "order of the side steps");
  k_rows<NS, IG + 1>(kb, rk, voff);
  {
    const size_t o = (size_t)(row0 + 2 * r8) * FF + (u0 + (c8 < nu ? c8 : nu - 1)) * 8;
    wa[0] = ld_nt((gp4)(wg_ + o)); wa[1] = ld_nt((gp4)(wg_ + o + FF)); wa[2] = ld_nt((gp4)(wu_ + o)); wa[3] = ld_nt((gp4)(wu_ + o + FF));
    SCHED_FENCE();
  }
  k_rows<IG + 1, IG + WIN + 1>(kb, rk, voff);
  {
    float ga[8], ua[8];
#pragma unroll
    for (int k = 0; k < 4; ++k) {
      ga[2 * k] = dot2(pair_lo(wa[0][k], wa[1][k]), xr, 0.f); ga[2 * k + 1] = dot2(pair_hi(wa[0][k], wa[1][k]), xr, 0.f);
      ua[2 * k] = dot2(pair_lo(wa[2][k], wa[3][k]), xr, 0.f); ua[2 * k + 1] = dot2(pair_hi(wa[2][k], wa[3][k]), xr, 0.f);
    }
    float g4[4], u4[4];
#pragma unroll
    for (int k = 0; k < 4; ++k) { g4[k] = tr32(ga[k], ga[k + 4]); u4[k] = tr32(ua[k], ua[k + 4]); }
    const float g0 = tr16(g4[0], g4[2]), g1 = tr16(g4[1], g4[3]), v0 = tr16(u4[0], u4[2]), v1 = tr16(u4[1], u4[3]);
    *(LAS f32x2*)(ldf + L_P1 + sw * 128 + 2 * (8 * c8 + r8)) = f32x2{trd<XOR8>(g0, g1, (lane & 8) != 0), trd<XOR8>(v0, v1, (lane & 8) != 0)};
    lds_post(flag + 4, sw, lane);
    SCHED_FENCE();
  }
  k_rows<IG + WIN + 1, IQ + 1>(kb, rk, voff);
  {
    gq[0] = ld_gran(rs, lane * 16, WS_HQ + (h * 8 + 2 * sw) * 1024); gq[1] = ld_gran(rs, lane * 16, WS_HQ + (h * 8 + 2 * sw + 1) * 1024);
    SCHED_FENCE();
  }
  k_rows<IQ + 1, IQ + WIN + 1>(kb, rk, voff);
  {
    gather_finish<2, 1024>(gq, rs, lane * 16, WS_HQ + (h * 8 + 2 * sw) * 1024, tagA, tagB);
    *(LAS f32x2*)(ldf + L_P2 + sw * 128 + 2 * lane) = f32x2{u2f(gq[0][0]) + u2f(gq[1][0]), u2f(gq[0][2]) + u2f(gq[1][2])};
    lds_post(flag + 8, sw, lane);
    SCHED_FENCE();
  }
  k_rows<IQ + WIN + 1, NBACK>(kb, rk, voff);
  lds_wait4(flag + 8, lane);
  u32x4 q;
  {
    typedef float f32x4 __attribute__((ext_vector_type(4)));
    f32x4 a = *(const LAS f32x4*)(ldf + L_P2 + 8 * c), d = *(const LAS f32x4*)(ldf + L_P2 + 8 * c + 4);
#pragma unroll
    for (int ww = 1; ww < 4; ++ww) { a += *(const LAS f32x4*)(ldf + L_P2 + ww * 128 + 8 * c); d += *(const LAS f32x4*)(ldf + L_P2 + ww * 128 + 8 * c + 4); }
    q = u32x4{pack_bf16(a[0], a[1]), pack_bf16(a[2], a[3]), pack_bf16(d[0], d[1]), pack_bf16(d[2], d[3])};
  }
  float sc[4], xs[16];
#pragma unroll
  for (int st = 0; st < 64 + WIN - NBACK; ++st) {
    const int n = NBACK + st;
    const int lim = n + 1 - WIN < CR * (st + 1) ? n + 1 - WIN : CR * (st + 1);
    const int from = st == 0 ? 0 : (n - WIN < CR * st ? n - WIN : CR * st);
    if (from < n - WIN) PACE(WIN - 1);
    if (n < 64) kb[n] = ld_kv(rk, voff, n * 4096); else vb[n - 64] = ld_kv(rv, voff, (n - 64) * 4096);
    SCHED_FENCE();
#pragma unroll
    for (int j = from; j < lim; ++j) {
      xs[j & 15] = qk_dot(kb[j], q);
      asm volatile("" : "+v"(xs[j & 15]));
      if ((j & 15) == 15) { sc[j >> 4] = score16(xs, c); asm volatile("" : "+v"(sc[j >> 4])); }
    }
    SCHED_FENCE();
  }
  static_assert(CR * (64 + WIN - NBACK) >= 64, "catch-up must finish inside the K phase");
  u32x4 wov[4], wdv[2], xg, gwv;
  const int gvoff = (lane < 8 * nu ? lane : 0) * 16;
  const gp4 pwo = (gp4)(wo_ + (size_t)(h * DH + 32 * sw + 2 * r8) * D + 64 * s + 8 * c8);
  const gp4 pwd0 = (gp4)(wd_ + (size_t)(8 * (u0 + sw) + c8) * D + 64 * s + 8 * r8);
  const gp4 pwd1 = (gp4)(wd_ + (size_t)(8 * (u0 + (sw + 4 < nu ? sw + 4 : sw)) + c8) * D + 64 * s + 8 * r8);
#pragma unroll
  for (int n = WIN; n < WIN + ES; ++n) { vb[n] = ld_kv(rv, voff, n * 4096); SCHED_FENCE(); }
  const float mw = fmaxf(xrow_max(row_max(fmaxf(fmaxf(sc[0], sc[1]), fmaxf(sc[2], sc[3])))), -3.0e38f);
  float p[4];
#pragma unroll
  for (int i = 0; i < 4; ++i) p[i] = __builtin_amdgcn_exp2f(sc[i] - mw);
  {
    const float lw = xrow_sum(row_sum((p[0] + p[1]) + (p[2] + p[3])));
    if (lane == 0) *(LAS f32x2*)(ldf + L_ML + 2 * sw) = f32x2{mw, lw};
    lds_post(flag + 12, sw, lane);
  }
  float acc[8];
#pragma unroll
  for (int k = 0; k < 8; ++k) acc[k] = 0.f;
#define SIDE(I, E, STMT) PVS<I, E>::run(vb, rv, voff, p, acc); PACE(WIN - 1); STMT; SCHED_FENCE();
  SIDE(0, 1, gq[0] = ld_gran(rs, gvoff, WS_HG + (h * 8 + 2 * sw) * 1024))
  SIDE(1, 2, gq[1] = ld_gran(rs, gvoff, WS_HG + (h * 8 + 2 * sw + 1) * 1024))
  SIDE(2, 3, if (sw == 0) { xg = ((gp4)x_)[lane]; gwv = ((gp4)gw_)[lane]; })
  SIDE(3, 4, wdv[0] = ld_nt(pwd0))
  SIDE(4, 5, wdv[1] = ld_nt(pwd1))
  SIDE(5, 6, wov[0] = ld_nt(pwo))
  SIDE(6, 7, wov[1] = ld_nt(pwo + D / 8))
  SIDE(7, 8, wov[2] = ld_nt(pwo + 16 * D / 8))
  SIDE(8, 9, wov[3] = ld_nt(pwo + 17 * D / 8))
  PVS<9, GP>::run(vb, rv, voff, p, acc);
  float gate = 0.f, omg = 0.f;
  {
    gather_finish<2, 1024>(gq, rs, gvoff, WS_HG + (h * 8 + 2 * sw) * 1024, tagA, tagB);
    *(LAS f32x2*)(ldf + L_P3 + sw * 128 + 2 * lane) = f32x2{u2f(gq[0][0]) + u2f(gq[1][0]), u2f(gq[0][2]) + u2f(gq[1][2])};
    lds_post(flag + 16, sw, lane);
    if (sw == 0) {
      float z = 0.f;
#pragma unroll
      for (int k = 0; k < 4; ++k) {
        z += rbf(bflo(xg[k]) * bflo(gwv[k]));
        z += rbf(bfhi(xg[k]) * bfhi(gwv[k]));
      }
      z = rbf(xrow_sum(row_sum(z)));
      gate = rbf(1.0f / (1.0f + __expf(-z)));
      omg = rbf(1.0f - gate);
    }
    asm volatile("" : "+v"(gate), "+v"(omg));
  }
  SCHED_FENCE();
  PVS<GP, YP>::run(vb, rv, voff, p, acc);
  unsigned wp[16];
#pragma unroll
  for (int k = 0; k < 4; ++k) {
    wp[2 * k] = pair_lo(wov[0][k], wov[1][k]); wp[2 * k + 1] = pair_hi(wov[0][k], wov[1][k]);
    wp[8 + 2 * k] = pair_lo(wov[2][k], wov[3][k]); wp[8 + 2 * k + 1] = pair_hi(wov[2][k], wov[3][k]);
  }
#pragma unroll
  for (int k = 0; k < 16; ++k) asm volatile("" : "+v"(wp[k]));
  u32x4 gm[1];
  gm[0] = ld_gran(rs, c8 * 128, WS_ML + h * 1024);
  {
    lds_wait4(flag + 16, lane);
    f32x2 t0 = *(const LAS f32x2*)(ldf + L_P3 + 2 * (8 * sw + c8)), t1 = *(const LAS f32x2*)(ldf + L_P3 + 2 * (8 * sw + 32 + c8));
#pragma unroll
    for (int ww = 1; ww < 4; ++ww) {
      t0 += *(const LAS f32x2*)(ldf + L_P3 + ww * 128 + 2 * (8 * sw + c8)); t1 += *(const LAS f32x2*)(ldf + L_P3 + ww * 128 + 2 * (8 * sw + 32 + c8));
    }
    const float h0 = rbf(rbf(t0.x) * rbf(t0.y)), h1 = sw + 4 < nu ? rbf(rbf(t1.x) * rbf(t1.y)) : 0.f;
    float y[8];
#pragma unroll
    for (int k = 0; k < 4; ++k) {
      y[2 * k] = fmaf(h0, bflo(wdv[0][k]), h1 * bflo(wdv[1][k]));
      y[2 * k + 1] = fmaf(h0, bfhi(wdv[0][k]), h1 * bfhi(wdv[1][k]));
    }
    ldf[L_Y + 256 + sw * 64 + lane] = tr8(y, c8);
  }
  SCHED_FENCE();
  PVS<YP, FP>::run(vb, rv, voff, p, acc);
  float f;
  int half;
  {
    gather_finish<1, 0>(gm, rs, c8 * 128, WS_ML + h * 1024, tagA, tagB);
    const float ms = u2f(gm[0][0]);
    float Mh = fmaxf(ms, dpp<XOR1>(ms)); Mh = fmaxf(Mh, dpp<XOR2>(Mh)); Mh = fmaxf(Mh, dpp<XOR7>(Mh));
    half = __builtin_popcountll(__builtin_amdgcn_ballot_w64((int)gm[0][2] < 0) & 0xffull) >= VOTES ? 0 : 1;
    if ((sw | lane) == 0) ldi[L_HALF] = half;
    half *= 8192;
    float Lh = __builtin_amdgcn_exp2f(ms - Mh) * fabsf(u2f(gm[0][2])); Lh += dpp<XOR1>(Lh); Lh += dpp<XOR2>(Lh); Lh += dpp<XOR7>(Lh);
    f = __builtin_amdgcn_exp2f(mw - Mh) / Lh;
    asm volatile("" : "+v"(f));
  }
  SCHED_FENCE();
  PVS<FP, 64>::run(vb, rv, voff, p, acc);
  {
    const float t0 = tr32(acc[0], acc[4]), t1 = tr32(acc[1], acc[5]), t2 = tr32(acc[2], acc[6]), t3 = tr32(acc[3], acc[7]);
    *(LAS f32x2*)(ldf + L_OP + sw * 128 + 8 * c + 2 * g) = f32x2{tr16(t0, t2) * f, tr16(t1, t3) * f};
  }
  __syncthreads();
  {
    u32x4 gr[2];
    gather<2, 128, 0>(gr, rs, c8 * 1024 + r8 * 16, WS_H1 + h * 16384 + half + sw * 256, tagA, tagB);
    float o[4] = {u2f(gr[0][0]), u2f(gr[0][2]), u2f(gr[1][0]), u2f(gr[1][2])};
#pragma unroll
    for (int k = 0; k < 4; ++k) o[k] += dpp<XOR1>(o[k]);
#pragma unroll
    for (int k = 0; k < 4; ++k) o[k] += dpp<XOR2>(o[k]);
#pragma unroll
    for (int k = 0; k < 4; ++k) o[k] += dpp<XOR7>(o[k]);
    const unsigned oa = pack_bf16(o[0], o[1]), ob = pack_bf16(o[2], o[3]);
    float y[8];
#pragma unroll
    for (int k = 0; k < 8; ++k) y[k] = dot2(wp[8 + k], ob, dot2(wp[k], oa, 0.f));
    const float t0 = tr32(y[0], y[4]), t1 = tr32(y[1], y[5]), t2 = tr32(y[2], y[6]), t3 = tr32(y[3], y[7]);
    ldf[L_Y + sw * 64 + 8 * c8 + r8] = trd<XOR8>(tr16(t0, t2), tr16(t1, t3), (lane & 8) != 0);
  }
  __syncthreads();
  if (sw == 0) {
    u32x4 gr[1];
    gather<1, 0, 0>(gr, rs, (lane >> 4) * 16384 + ((lane >> 1) & 7) * 1024 + (lane & 1) * 16, WS_H2 + s * 65536 + half + h * 32, tagA, tagB);
    float as = u2f(gr[0][0]), bs = u2f(gr[0][2]);
    as += dpp<XOR2>(as); bs += dpp<XOR2>(bs);
    as += dpp<ROR4>(as); bs += dpp<ROR4>(bs);
    as += dpp<XOR8>(as); bs += dpp<XOR8>(bs);
    as = rbf(xrow_sum(as)); bs = rbf(xrow_sum(bs));
    const float res = rbf(gate * as) + rbf(omg * bs);
    const unsigned o2 = pack_bf16(res, dpp<XOR1>(res));
    if (lane == 0) {
      *(GAS unsigned*)(out_ + 64 * s + 2 * h) = o2;
      if (b == 0) __builtin_amdgcn_raw_buffer_store_b64(u32x2{epA, epB}, rs, 0, WS_EPOCH, AUX_SC1);
    }
  }
}

extern "C" void kernel_launch(void* const* d_in, const int* in_sizes, int n_in, void* d_out, int out_size, void* d_ws,
                              size_t ws_size, hipStream_t stream) {
  if (n_in != 10 || in_sizes[1] != NH * SK * DH || out_size != D || ws_size < (size_t)WS_OOB + (6u << 20)) return;
  typedef const unsigned short* cp;
  Params p;
  p.x = (cp)d_in[0]; p.k = (cp)d_in[1]; p.v = (cp)d_in[2]; p.wq = (cp)d_in[3]; p.wo = (cp)d_in[4];
  p.wg = (cp)d_in[6]; p.wu = (cp)d_in[7]; p.wd = (cp)d_in[8]; p.gw = (cp)d_in[9];
  p.out = (unsigned short*)d_out; p.ws = (unsigned char*)d_ws;
  fused_kernel<<<NWG, NTHR, 0, stream>>>(p);
}
```
